```python
import torch

def _softmax(x, axis=-1):
    return torch.softmax(x, dim=axis)
def _gelu(x):
    return torch.nn.functional.gelu(x, approximate="tanh")
def _silu(x):
    return torch.nn.functional.silu(x)
def _sigmoid(x):
    return torch.sigmoid(x)
def _layernorm(x, axis=-1, eps=1e-5):
    m = x.mean(dim=axis, keepdim=True)
    v = x.var(dim=axis, keepdim=True, unbiased=False)
    return (x - m) / torch.sqrt(v + eps)
def _scatter_add(tgt, idx, src, axis=0):
    return tgt.clone().index_add_(axis, idx, src)
def _segment_sum(src, seg, n):
    out = torch.zeros((n,) + src.shape[1:], dtype=src.dtype)
    return out.index_add_(0, seg, src)
def _pad(x, pw):
    flat = tuple(v for pair in reversed(pw) for v in pair)
    return torch.nn.functional.pad(x, flat)

kernel_name = 'concat_heads__gqa_block_cached__gqa_block_cached__near_ridge'

def setup_inputs():

    _w = (lambda a: (lambda t: t.to(torch.bfloat16) if t.is_floating_point() else t)(torch.as_tensor(a)))
    def _A_setup():
        B, Sq, D = 8, 96, 640
        Hq, Hkv, Sk, Dh = 32, 32, 96, 128
        assert Hq % Hkv == 0, (Hq, Hkv)
        return {
            "x": _w((lambda *s: torch.randn(*s, dtype=torch.bfloat16) * 0.8)(B, Sq, D)),
            "k_cache": _w((lambda *s: torch.randn(*s, dtype=torch.bfloat16) * 0.8)(B, Hkv, Sk, Dh)),
            "v_cache": _w((lambda *s: torch.randn(*s, dtype=torch.bfloat16) * 0.05)(B, Hkv, Sk, Dh)),
            "wq": _w((lambda *s: torch.randn(*s, dtype=torch.bfloat16) * 0.8)(D, Hq * Dh) * (D ** -0.5)),
            "wo": _w((lambda *s: torch.randn(*s, dtype=torch.bfloat16) * 0.05)(Hq * Dh, D) * ((Hq * Dh) ** -0.5)),
        }
    _A_in = _A_setup()

    def _B_setup():
        B, Sq, D = 8, 96, 640
        Hq, Hkv, Sk, Dh = 32, 32, 96, 128
        assert Hq % Hkv == 0, (Hq, Hkv)
        return {
            "x": _w((lambda *s: torch.randn(*s, dtype=torch.bfloat16) * 0.8)(B, Sq, D)),
            "k_cache": _w((lambda *s: torch.randn(*s, dtype=torch.bfloat16) * 0.8)(B, Hkv, Sk, Dh)),
            "v_cache": _w((lambda *s: torch.randn(*s, dtype=torch.bfloat16) * 0.05)(B, Hkv, Sk, Dh)),
            "wq": _w((lambda *s: torch.randn(*s, dtype=torch.bfloat16) * 0.8)(D, Hq * Dh) * (D ** -0.5)),
            "wo": _w((lambda *s: torch.randn(*s, dtype=torch.bfloat16) * 0.05)(Hq * Dh, D) * ((Hq * Dh) ** -0.5)),
        }
    _B_in = _B_setup()

    return {
        "A_x": _A_in["x"],
        "A_k_cache": _A_in["k_cache"],
        "A_v_cache": _A_in["v_cache"],
        "A_wq": _A_in["wq"],
        "A_wo": _A_in["wo"],
        "B_x": _B_in["x"],
        "B_k_cache": _B_in["k_cache"],
        "B_v_cache": _B_in["v_cache"],
        "B_wq": _B_in["wq"],
        "B_wo": _B_in["wo"],
    }

def reference(A_x, A_k_cache, A_v_cache, A_wq, A_wo, B_x, B_k_cache, B_v_cache, B_wq, B_wo):
    def _A_ref(x, k_cache, v_cache, wq, wo):
        B, Sq, D = x.shape
        Hq, Dh = 32, 128
        Hkv = k_cache.shape[1]
        rep = Hq // Hkv
        q = (lambda x, *d: x.permute(*d))((x @ wq).reshape(B, Sq, Hq, Dh), 0, 2, 1, 3)
        k = (lambda x, n, axis: torch.repeat_interleave(x, n, dim=axis))(k_cache, rep, axis=1)
        v = (lambda x, n, axis: torch.repeat_interleave(x, n, dim=axis))(v_cache, rep, axis=1)
        scores = torch.einsum("bhqd,bhkd->bhqk", q, k) * (Dh ** -0.5)
        p = _softmax(scores, axis=-1)
        o = torch.einsum("bhqk,bhkd->bhqd", p, v)
        return (lambda x, *d: x.permute(*d))(o, 0, 2, 1, 3).reshape(B, Sq, Hq * Dh) @ wo

    def _B_ref(x, k_cache, v_cache, wq, wo):
        B, Sq, D = x.shape
        Hq, Dh = 32, 128
        Hkv = k_cache.shape[1]
        rep = Hq // Hkv
        q = (lambda x, *d: x.permute(*d))((x @ wq).reshape(B, Sq, Hq, Dh), 0, 2, 1, 3)
        k = (lambda x, n, axis: torch.repeat_interleave(x, n, dim=axis))(k_cache, rep, axis=1)
        v = (lambda x, n, axis: torch.repeat_interleave(x, n, dim=axis))(v_cache, rep, axis=1)
        scores = torch.einsum("bhqd,bhkd->bhqk", q, k) * (Dh ** -0.5)
        p = _softmax(scores, axis=-1)
        o = torch.einsum("bhqk,bhkd->bhqd", p, v)
        return (lambda x, *d: x.permute(*d))(o, 0, 2, 1, 3).reshape(B, Sq, Hq * Dh) @ wo

    a = _A_ref(A_x, A_k_cache, A_v_cache, A_wq, A_wo)
    b = _B_ref(B_x, B_k_cache, B_v_cache, B_wq, B_wo)
    return torch.concatenate([a, b], axis=-1)
```

```cpp
#include <hip/hip_runtime.h>
#include <hip/hip_bf16.h>
typedef __hip_bfloat16 bf16;
static constexpr int B_=8, SQ=96, D_=640, H_=32, SK=96, DH=128;
__device__ inline float b2f(bf16 x){ return __bfloat162float(x);}
__global__ __launch_bounds__(256) void k1(const bf16* x0,const bf16* k0,const bf16* v0,const bf16* wq0,
                   const bf16* x1,const bf16* k1_,const bf16* v1,const bf16* wq1, bf16* O){
  __shared__ float q[SQ][DH+1];
  __shared__ float p[SQ][SK+1];
  int id=blockIdx.x; int br=id/256, b=(id/32)%8, h=id%32;
  const bf16* x=(br?x1:x0)+(size_t)b*SQ*D_;
  const bf16* k=(br?k1_:k0)+((size_t)b*H_+h)*SK*DH;
  const bf16* v=(br?v1:v0)+((size_t)b*H_+h)*SK*DH;
  const bf16* wq=(br?wq1:wq0)+h*DH;
  int t=threadIdx.x;
  for(int e=t;e<SQ*DH;e+=256){int r=e/DH,c=e%DH; float s=0; for(int d=0;d<D_;d++) s+=b2f(x[r*D_+d])*b2f(wq[(size_t)d*H_*DH+c]); q[r][c]=b2f(__float2bfloat16(s));}
  __syncthreads();
  for(int e=t;e<SQ*SK;e+=256){int r=e/SK,c=e%SK; float s=0; for(int d=0;d<DH;d++) s+=q[r][d]*b2f(k[c*DH+d]); p[r][c]=s*0.08838834764831845f;}
  __syncthreads();
  if(t<SQ){float m=-1e30f; for(int c=0;c<SK;c++) m=fmaxf(m,p[t][c]); float l=0; for(int c=0;c<SK;c++){float e=__expf(p[t][c]-m); p[t][c]=e; l+=e;} float il=1.f/l; for(int c=0;c<SK;c++) p[t][c]=b2f(__float2bfloat16(p[t][c]*il));}
  __syncthreads();
  for(int e=t;e<SQ*DH;e+=256){int r=e/DH,c=e%DH; float s=0; for(int j=0;j<SK;j++) s+=p[r][j]*b2f(v[j*DH+c]); O[((size_t)br*768+b*SQ+r)*4096+h*DH+c]=__float2bfloat16(s);}
}
__global__ __launch_bounds__(256) void k2(const bf16* O,const bf16* wo0,const bf16* wo1,bf16* out){
  int e=blockIdx.x*256+threadIdx.x;
  int br=e/(768*640); int r=(e/640)%768; int c=e%640;
  const bf16* wo=br?wo1:wo0; const bf16* o=O+((size_t)br*768+r)*4096;
  float s=0; for(int k=0;k<4096;k++) s+=b2f(o[k])*b2f(wo[(size_t)k*640+c]);
  out[(size_t)r*1280+br*640+c]=__float2bfloat16(s);
}
extern "C" void kernel_launch(void* const* d_in, const int* in_sizes, int n_in, void* d_out, int out_size, void* d_ws, size_t ws_size, hipStream_t stream){
  const bf16* const* in=(const bf16* const*)d_in;
  bf16* O=(bf16*)d_ws;
  k1<<<512,256,0,stream>>>(in[0],in[1],in[2],in[3],in[5],in[6],in[7],in[8],O);
  k2<<<2*768*640/256,256,0,stream>>>(O,in[4],in[9],(bf16*)d_out);
}
```

```cpp
#include <hip/hip_runtime.h>
#include <hip/hip_bf16.h>
#include <utility>

typedef __bf16   bf16x8 __attribute__((ext_vector_type(8)));
typedef __bf16   bf16x4 __attribute__((ext_vector_type(4)));
typedef __bf16   bf16x2 __attribute__((ext_vector_type(2)));
typedef float    f32x4  __attribute__((ext_vector_type(4)));
typedef float    f32x2  __attribute__((ext_vector_type(2)));
typedef unsigned u32x4  __attribute__((ext_vector_type(4)));
typedef unsigned u32x2  __attribute__((ext_vector_type(2)));
typedef unsigned long long u64;
#define LAS __attribute__((address_space(3)))
#define GAS __attribute__((address_space(1)))
#define DI __device__ __forceinline__
typedef __amdgpu_buffer_rsrc_t rsrc_t;

template <int... Is, class F> DI void static_for_impl(std::integer_sequence<int, Is...>, F&& f) { (f(std::integral_constant<int, Is>{}), ...); }
template <int N, class F> DI void static_for(F&& f) { static_for_impl(std::make_integer_sequence<int, N>{}, f); }

constexpr int SQ = 96, DM = 640, NH = 32, DH = 128, HD = 4096;
constexpr unsigned WS_EPOCH = 0;
constexpr unsigned WS_FO    = 4096;
constexpr unsigned WS_FP    = WS_FO + 16 * 64 * 128;
constexpr unsigned WS_O     = 1u << 20;
constexpr unsigned WS_O_BYTES = 2 * 8 * 32 * 2 * 12288;
constexpr unsigned WS_P     = WS_O + WS_O_BYTES;
constexpr unsigned WS_P_BYTES = 16 * 4 * 4 * 4 * 15360;
constexpr unsigned WS_STAMP = 64u << 20;
constexpr unsigned L_WQ = 0;
constexpr unsigned L_X  = 65536;
constexpr unsigned L_KA = 102400, L_VA = 126976, L_SP = 151552, L_KB = 0, L_VB = 24576;
constexpr unsigned L_SYNC = L_SP + 10240;
constexpr unsigned LDS_BYTES = 163840;
#define NT_W 1
#define NT_X 0

DI unsigned rfl(unsigned x) { return __builtin_amdgcn_readfirstlane(x); }
DI unsigned cvtpk(float lo, float hi) { f32x2 v = {lo, hi}; bf16x2 b = __builtin_convertvector(v, bf16x2); return __builtin_bit_cast(unsigned, b); }
DI bf16x8 pack8(const f32x4& a, const f32x4& b) { u32x4 w = {cvtpk(a[0], a[1]), cvtpk(a[2], a[3]), cvtpk(b[0], b[1]), cvtpk(b[2], b[3])}; return __builtin_bit_cast(bf16x8, w); }
DI f32x4 mfma(bf16x8 a, bf16x8 b, f32x4 c) { return __builtin_amdgcn_mfma_f32_16x16x32_bf16(a, b, c, 0, 0, 0); }
DI bf16x4 trrd(unsigned addr) { return __builtin_amdgcn_ds_read_tr16_b64_v4bf16((LAS bf16x4*)addr); }
DI bf16x8 cat(bf16x4 a, bf16x4 b) { return __builtin_shufflevector(a, b, 0, 1, 2, 3, 4, 5, 6, 7); }
template <bool NT> DI void dma(unsigned lds, unsigned voff, rsrc_t r, unsigned soff) {
  if constexpr (NT) asm volatile("s_mov_b32 m0, %0\n\ts_nop 0\n\tbuffer_load_dwordx4 %1, %2, %3 offen nt lds" :: "s"(lds), "v"(voff), "s"(r), "s"(soff) : "memory");
  else              asm volatile("s_mov_b32 m0, %0\n\ts_nop 0\n\tbuffer_load_dwordx4 %1, %2, %3 offen lds" :: "s"(lds), "v"(voff), "s"(r), "s"(soff) : "memory");
}
template <int N> DI void wait_vm() { asm volatile("s_waitcnt vmcnt(%0)" :: "i"(N) : "memory"); }
#define SB() __builtin_amdgcn_sched_barrier(0)
template <int N> DI void wait_vm_bar() { SB(); __builtin_amdgcn_s_waitcnt(0xC07F); asm volatile("s_waitcnt vmcnt(%0)\n\ts_barrier" :: "i"(N > 63 ? 63 : N) : "memory"); SB(); }
template <int OFF> DI void ld_sc1(bf16x8& d, unsigned voff, rsrc_t r, unsigned soff) { asm volatile("buffer_load_dwordx4 %0, %1, %2, %3 offen offset:%4 sc1" : "=v"(d) : "v"(voff), "s"(r), "s"(soff), "i"(OFF) : "memory"); }
template <int OFF, class T> DI void st_sc1(const T& d, unsigned voff, rsrc_t r, unsigned soff) { asm volatile("buffer_store_dwordx4 %0, %1, %2, %3 offen offset:%4 sc1\n\ts_nop 1" :: "v"(d), "v"(voff), "s"(r), "s"(soff), "i"(OFF) : "memory"); }
DI void flag_ld(u64& d, unsigned voff, rsrc_t r) { asm volatile("buffer_load_dwordx2 %0, %1, %2, 0 offen sc1" : "=v"(d) : "v"(voff), "s"(r) : "memory"); }
DI void flag_st(u64 d, unsigned voff, rsrc_t r) { asm volatile("buffer_store_dwordx2 %0, %1, %2, 0 offen sc1" :: "v"(d), "v"(voff), "s"(r) : "memory"); }
DI void flag_spin(u64 v, unsigned voff, rsrc_t r, u64 tag) {
  unsigned cnt = 1u << 20;
  asm volatile("1:\n\tv_cmp_eq_u64 vcc, %[tag], %[v]\n\ts_nop 1\n\ts_cmp_eq_u64 vcc, exec\n\ts_cbranch_scc1 2f\n\ts_sub_u32 %[cnt], %[cnt], 1\n\ts_cbranch_scc1 2f\n\ts_sleep 1\n\t"
               "buffer_load_dwordx2 %[v], %[off], %[r], 0 offen sc1\n\ts_waitcnt vmcnt(0)\n\ts_branch 1b\n2:"
               : [v] "+v"(v), [cnt] "+s"(cnt) : [tag] "s"(tag), [off] "v"(voff), [r] "s"(r) : "vcc", "scc", "memory");
}
template <int N> DI void pair_sync(unsigned par, unsigned mine, unsigned other, unsigned val) {
  unsigned cnt = 1u << 22, t;
  asm volatile("s_cmp_eq_u32 %[par], 0\n\ts_cbranch_scc1 2f\n\ts_waitcnt vmcnt(%[n])\n\tds_write_b32 %[me], %[val]\n"
               "1:\n\tds_read_b32 %[t], %[ot]\n\ts_waitcnt lgkmcnt(0)\n\tv_cmp_ge_u32 vcc, %[t], %[val]\n\ts_nop 1\n\ts_cmp_eq_u64 vcc, exec\n\ts_cbranch_scc1 2f\n\ts_sub_u32 %[cnt], %[cnt], 1\n\ts_cbranch_scc0 1b\n2:"
               : [t] "=&v"(t), [cnt] "+s"(cnt) : [par] "s"(par), [me] "v"(mine), [ot] "v"(other), [val] "v"(val), [n] "i"(N) : "vcc", "scc", "memory");
}
template <int N> using IC = std::integral_constant<int, N>;
template <class T> DI void pin3(T (&a)[3]) { asm volatile("" : "+v"(a[0]), "+v"(a[1]), "+v"(a[2])); }
template <int N, class T> DI void pin(T (&a)[N][3]) { static_for<N>([&](auto i) { pin3(a[i]); }); }
DI float xmax16(float v) { auto r = __builtin_amdgcn_permlane16_swap(__float_as_uint(v), __float_as_uint(v), false, false); return fmaxf(__uint_as_float(r[0]), __uint_as_float(r[1])); }
DI float xmax32(float v) { auto r = __builtin_amdgcn_permlane32_swap(__float_as_uint(v), __float_as_uint(v), false, false); return fmaxf(__uint_as_float(r[0]), __uint_as_float(r[1])); }
DI float xsum16(float v) { auto r = __builtin_amdgcn_permlane16_swap(__float_as_uint(v), __float_as_uint(v), false, false); return __uint_as_float(r[0]) + __uint_as_float(r[1]); }
DI float xsum32(float v) { auto r = __builtin_amdgcn_permlane32_swap(__float_as_uint(v), __float_as_uint(v), false, false); return __uint_as_float(r[0]) + __uint_as_float(r[1]); }
DI rsrc_t mkrsrc(const void* p, unsigned bytes) { return __builtin_amdgcn_make_buffer_rsrc((void*)p, 0, bytes, 0x00020000); }
DI void poll_flags(const char* ws, unsigned off, bool need, u64 tag) {
  for (unsigned spins = 0; spins < (1u << 20); ++spins) {
    u64 v = tag;
    if (need) v = __hip_atomic_load((const GAS u64*)(ws + off), __ATOMIC_RELAXED, __HIP_MEMORY_SCOPE_AGENT);
    if (__all(v == tag)) break;
    __builtin_amdgcn_s_sleep(1);
  }
}
#define STAMP(i)
#define STAMP_DECL
#define CSTAMP(i)
#define STAMP_OUT

struct Params { const char *x[2], *k[2], *v[2], *wq[2], *wo[2]; char* out; char* ws; };

#define ABL_NODMA 0
#define ABL_NOLDS 0
#define ABL_NOBAR 0
#define S_KA 8
#define S_VA 12
__global__ __launch_bounds__(256, 2) void fused(Params p) {
  extern __shared__ __attribute__((aligned(16))) char smem[];
  const unsigned tid = threadIdx.x, lane = tid & 63u, w = rfl(tid >> 6);
  const unsigned id = blockIdx.x, xcd = id & 7u, slot = id >> 3, br = xcd >> 2, hg = xcd & 3u, b = slot >> 2, hp = slot & 3u;
  const unsigned par = w >> 1, qh = w & 1u, hA = 8 * hg + 2 * hp, cluster = br * 8 + b;
  const unsigned g = lane >> 4, c = lane & 15u, q4 = c >> 2, pp = lane & 3u;
  char* ws = p.ws;
  constexpr unsigned L0 = 0;
  STAMP_DECL;
  STAMP(0);
  const u64 tag = *(const u64*)(ws + WS_EPOCH + cluster * 256) + 1;

  const rsrc_t rX  = mkrsrc(p.x[br] + (size_t)b * SQ * DM * 2, SQ * DM * 2);
  const rsrc_t rWq = mkrsrc(p.wq[br] + hA * DH * 2, DM * HD * 2 - hA * DH * 2);
  const rsrc_t rK  = mkrsrc(p.k[br] + ((size_t)b * NH + hA) * SQ * DH * 2, 2 * SQ * DH * 2);
  const rsrc_t rV  = mkrsrc(p.v[br] + ((size_t)b * NH + hA) * SQ * DH * 2, 2 * SQ * DH * 2);
  const rsrc_t rWo = mkrsrc(p.wo[br] + (size_t)hg * 1024 * DM * 2 + hp * 320, 1024 * DM * 2 - hp * 320);
  const rsrc_t rWs = mkrsrc(ws, WS_P + WS_P_BYTES);

  const unsigned hi = lane >> 5, c32 = lane & 31u;
  unsigned vWq[2];
  { const unsigned r3 = (2 * w + hi) & 3u;
    vWq[0] = (2 * w + hi) * 8192 + ((c32 ^ (r3 << 1)) << 4);
    vWq[1] = (2 * w + hi) * 8192 + ((c32 ^ ((r3 | 4u) << 1)) << 4); }
  const unsigned vX = (8 * w + (lane >> 3)) * 1280 + (((lane & 7u) ^ ((4 * (w & 1u) + (lane >> 4)) & 7u)) << 4);
  const unsigned vK = (4 * w + g) * 256 + ((c ^ ((4 * w + g) & 15u)) << 4);
  const unsigned vV = (4 * w + g) * 256 + ((c ^ (((4 * w + g) & 7u) << 1)) << 4);
  const unsigned wk = w * 1024;
  unsigned vKB[2];
  vKB[0] = (4 * qh + g) * 256 + ((c ^ (4 * qh + g)) << 4); vKB[1] = (4 * qh + 8 + g) * 256 + ((c ^ (4 * qh + 8 + g)) << 4);
  const unsigned vVB = (4 * qh + g) * 256 + ((c ^ (((4 * qh + g) & 7u) << 1)) << 4);
  if (tid < 4) *(LAS unsigned*)(L_SYNC + tid * 4) = 0;

  auto pieceU = [&](auto s_, auto j) { constexpr int s = s_; dma<NT_W>(wk + (L_WQ + (s & 3) * 16384 + 4096 * j), vWq[j & 1], rWq, (unsigned)(32 * s + 8 * j) * 8192); };
  auto pieceE = [&](auto e_, auto j) { constexpr int e = e_; dma<NT_X>(wk + (L_X + (e % 3) * 12288 + 4096 * j), vX, rX, (unsigned)(32 * j * 1280 + 128 * e)); };
  auto issueU = [&](auto s_) { static_for<4>([&](auto j) { pieceU(s_, j); }); };
  auto issueE = [&](auto e_) { static_for<3>([&](auto j) { pieceE(e_, j); }); };

  const unsigned F = q4 | ((g & 1u) << 2);
  unsigned aWq[8];
  static_for<8>([&](auto t) { aWq[t] = (L_WQ + (8 * g + q4) * 512 + par * 256 + F * 32 + 8 * pp) ^ (t << 5); });
  unsigned aX[2];
  aX[0] = L_X + (48 * qh + c) * 128 + ((g ^ ((c >> 1) & 7u)) << 4); aX[1] = aX[0] ^ 64u;

  issueU(IC<0>{}); issueE(IC<0>{}); issueU(IC<1>{}); issueE(IC<1>{}); issueU(IC<2>{}); issueU(IC<3>{});
  f32x4 acc[8][3];
  static_for<8>([&](auto t) { static_for<3>([&](auto u) { acc[t][u] = f32x4{0, 0, 0, 0}; }); });
  bf16x8 fa[8], fb[2][3];
  struct Sch { int cum[22]; int endU[21]; };
  constexpr Sch sch = [] { Sch r{}; int n = 0;
    n += 7; r.endU[0] = n; n += 4; r.endU[1] = n; n += 7; r.endU[2] = n; n += 4; r.endU[3] = n;
    for (int s = 0; s <= 20; ++s) { r.cum[s] = n;
      if (s + 4 < 20) { if ((s & 1) == 0) n += 3; n += 4; r.endU[s + 4] = n; }
      if (s == S_KA || s == S_VA) n += 6; }
    r.cum[21] = n; return r; }();
  wait_vm_bar<sch.cum[0] - sch.endU[0]>();
  STAMP(1); CSTAMP(28);
  static_for<3>([&](auto u) { fb[0][u] = *(LAS bf16x8*)(aX[0] + u * 2048); if (ABL_NOLDS) fb[1][u] = *(LAS bf16x8*)(aX[1] + u * 2048); });
  static_for<8>([&](auto t) { fa[t] = cat(trrd(aWq[t]), trrd(aWq[t] + 2048)); });
  static_for<20>([&](auto s_) { constexpr int s = s_, n = s + 1;
    if constexpr (s == 18) { if (par) wait_vm_bar<sch.cum[s] - sch.endU[n] + 12>(); else wait_vm_bar<sch.cum[s] - sch.endU[n]>(); }
    else if constexpr (n < 20 && !ABL_NOBAR) wait_vm_bar<sch.cum[s] - sch.endU[n]>();
    static_for<8>([&](auto t_) { constexpr int t = t_;
      acc[t][0] = mfma(fa[t], fb[s & 1][0], acc[t][0]); SB();
      if constexpr (n < 20 && t < 3 && !ABL_NOLDS) { fb[n & 1][t] = *(LAS bf16x8*)(aX[n & 1] + ((n >> 1) % 3) * 12288 + t * 2048); SB(); }
      if constexpr (s + 4 < 20 && !ABL_NODMA) {
        if constexpr ((s & 1) == 0) { if constexpr (t < 3) pieceE(IC<(s + 4) / 2>{}, IC<t>{}); else if constexpr (t < 7) pieceU(IC<s + 4>{}, IC<t - 3>{}); }
        else if constexpr (t < 4) pieceU(IC<s + 4>{}, IC<t>{});
        SB(); }
      acc[t][1] = mfma(fa[t], fb[s & 1][1], acc[t][1]); SB();
      acc[t][2] = mfma(fa[t], fb[s & 1][2], acc[t][2]); SB();
      if constexpr (n < 20 && !ABL_NOLDS) { const unsigned a = aWq[t] + (n & 3) * 16384; fa[t] = cat(trrd(a), trrd(a + 2048)); SB(); }
    });
    if constexpr (s == S_KA) static_for<6>([&](auto j) { dma<NT_W>(wk + (L_KA + 4096 * j), vK, rK, j * 4096); });
    if constexpr (s == S_VA) static_for<6>([&](auto j) { dma<NT_W>(wk + (L_VA + 4096 * j), vV, rV, j * 4096); });
    if constexpr (s == 17) { if (par) static_for<12>([&](auto j) { dma<NT_W>(qh * 1024 + (L_KB + 2048 * j), vKB[j & 1], rK, 24576 + (j >> 1) * 4096); }); }
    if constexpr (s == 18) { if (par) static_for<12>([&](auto j) { dma<NT_W>(qh * 1024 + (L_VB + 2048 * j), vVB, rV, 24576 + j * 2048); }); }
  });
  pin(acc);
  STAMP(2); CSTAMP(29);
  constexpr int NZ = 7;
  constexpr unsigned ZU[NZ][2] = {{49152, 59392}, {69632, 79872}, {90112, L_SP}, {L_KA, L_KA + 10240}, {L_KA + 20480, L_KA + 30720}, {0, 10240}, {20480, 30720}};
  unsigned vWo[5];
  static_for<5>([&](auto i) { const unsigned e = i * 64 + lane, row = e / 20, pos = e - row * 20; vWo[i] = (row + 16 * qh) * 1280 + ((pos ^ (((row >> 2) & 1u) << 1)) << 4); });
  auto p2_row = [&](int i, unsigned pr) -> unsigned {
    if (i < 4) return (2 * hp + pr) * 4 + i; const unsigned a = (i - 4) % 6 + 6 * pr; return (2 * ((hp + 1 + (a >> 2)) & 3u) + (i >= 10 ? 1u : 0u)) * 4 + (a & 3u); };
  auto pieceZ = [&](auto i_, unsigned pr, unsigned ld, auto j) { constexpr int i = i_; dma<NT_W>(ld + j * 1024, vWo[j], rWo, p2_row(i, pr) * (32 * 1280)); };
  wait_vm_bar<63>();
  if (par) static_for<3>([&](auto i) { static_for<2>([&](auto pr) { static_for<5>([&](auto j) { pieceZ(i, pr, ZU[i][pr] + qh * 5120, j); }); }); });

  bf16x8 qf[4][3];
  static_for<4>([&](auto s) { static_for<3>([&](auto u) { qf[s][u] = pack8(acc[2 * s][u], acc[2 * s + 1][u]); }); });
  SB(); pair_sync<42>(par, L_SYNC + w * 4, L_SYNC + (w ^ 1u) * 4, 1); SB();
  STAMP(3);
  f32x4 st[6][3];
  { const unsigned aK0 = (par ? L_KB : L_KA) + c * 256 + (((g >> 1) ^ c) << 4) + 8 * (g & 1u);
    bf16x8 kf[4];
    auto rdK = [&](auto i_) { constexpr int i = i_, kt = i >> 2, s = i & 3;
      kf[s] = cat(*(LAS bf16x4*)((aK0 ^ (64 * s)) + kt * 4096), *(LAS bf16x4*)((aK0 ^ (64 * s + 32)) + kt * 4096)); };
    static_for<4>([&](auto i) { rdK(i); });
    SB();
    static_for<24>([&](auto i_) { constexpr int i = i_, kt = i >> 2, s = i & 3;
      static_for<3>([&](auto u) { st[kt][u] = mfma(kf[s], qf[s][u], s == 0 ? f32x4{0, 0, 0, 0} : st[kt][u]); });
      SB();
      if constexpr (i + 4 < 24) { rdK(IC<i + 4>{}); SB(); } }); }
  bf16x8 pf[3][3];
  constexpr float CS = 0.08838834764831845f * 1.4426950408889634f;
  static_for<3>([&](auto u) {
    float m = st[0][u][0];
    static_for<6>([&](auto kt) { static_for<4>([&](auto r) { m = fmaxf(m, st[kt][u][(int)r]); }); });
    m = xmax32(xmax16(m));
    const float mc = -m * CS; float l = 0.f;
    static_for<6>([&](auto kt) { static_for<4>([&](auto r) { const float e = __builtin_amdgcn_exp2f(fmaf(st[kt][u][(int)r], CS, mc)); st[kt][u][(int)r] = e; l += e; }); });
    l = xsum32(xsum16(l));
    const float il = __builtin_amdgcn_rcpf(l);
    static_for<3>([&](auto s) { pf[s][u] = pack8(st[2 * s][u] * il, st[2 * s + 1][u] * il); });
  });
  STAMP(4);
  SB(); pair_sync<30>(par, L_SYNC + w * 4, L_SYNC + (w ^ 1u) * 4, 2); SB();
  STAMP(5);
  const unsigned fA = WS_FO + (((cluster * 4 + hg) * 4 + ((hp + 1 + ((6 * par + (lane & 1u) * 5) >> 2)) & 3u)) * 4 + qh) * 128, fB = fA + 256;
  bf16x8 of[4][3];
  { f32x4 ot[8][3];
    const unsigned aV0 = (par ? L_VB : L_VA) + (4 * g + q4) * 256 + F * 32 + 8 * pp;
    bf16x8 vf[4];
    auto rdV = [&](auto i_) { constexpr int i = i_, t = i / 3, s = i % 3; const unsigned a = (aV0 ^ (t << 5)) + s * 8192; vf[i & 3] = cat(trrd(a), trrd(a + 4096)); };
    static_for<4>([&](auto i) { rdV(i); });
    SB();
    const unsigned ob = WS_O + ((cluster * 32 + hA + par) * 2 + qh) * 12288;
    static_for<24>([&](auto i_) { constexpr int i = i_, t = i / 3, s = i % 3;
      static_for<3>([&](auto u) { ot[t][u] = mfma(vf[i & 3], pf[s][u], s == 0 ? f32x4{0, 0, 0, 0} : ot[t][u]); });
      SB();
      if constexpr (i + 4 < 24) { rdV(IC<i + 4>{}); SB(); }
      if constexpr (i >= 8 && (i - 8) % 6 < 3) { constexpr int k = (i - 8) / 6, u = (i - 8) % 6;
        of[k][u] = pack8(ot[2 * k][u], ot[2 * k + 1][u]); st_sc1<u * 1024>(of[k][u], lane * 16, rWs, ob + k * 3072); SB(); } });
    static_for<3>([&](auto u) { of[3][u] = pack8(ot[6][u], ot[7][u]); st_sc1<u * 1024>(of[3][u], lane * 16, rWs, ob + 3 * 3072); });
  }
  STAMP(6);
  u64 fv;
  flag_ld(fv, fA, rWs);
  wait_vm<1>();
  if (lane == 0) flag_st(tag, WS_FO + (((cluster * 4 + hg) * 4 + hp) * 4 + w) * 128, rWs);
  STAMP(7);
  asm volatile("s_waitcnt vmcnt(1)" : "+v"(fv) :: "memory");
  flag_spin(fv, fA, rWs, tag);
  STAMP(8);
  bf16x8 ob[5][3];
  auto ldO = [&](auto i_) { constexpr int i = i_; const unsigned rw = p2_row(i, par), so = WS_O + ((cluster * 32 + 8 * hg + (rw >> 2)) * 2 + qh) * 12288 + (rw & 3u) * 3072;
    static_for<3>([&](auto u) { ld_sc1<u * 1024>(ob[(i - 4) % 5][u], lane * 16, rWs, so); }); };
  ldO(IC<4>{});
  wait_vm_bar<3>();
  STAMP(9); CSTAMP(30);
  const unsigned zq = qh * 5120;
  static_for<3>([&](auto k) { static_for<5>([&](auto j) { pieceZ(IC<3 + k>{}, par, (par ? ZU[3 + k][1] : ZU[3 + k][0]) + zq, j); }); });
  f32x4 a2[10][3];
  const unsigned aZe = (4 * g + q4) * 320 + (g & 1u) * 32 + 8 * pp, aZo = (4 * g + q4) * 320 - (g & 1u) * 32 + 8 * pp;
  bf16x8 af[5];
  auto rdZ = [&](auto f_) { constexpr int f = f_, i = f / 10, t = f % 10; const unsigned a = ((t & 1) ? aZo : aZe) + (par ? ZU[i % NZ][1] : ZU[i % NZ][0]) + t * 32; af[f % 5] = cat(trrd(a), trrd(a + 5120)); };
  static_for<5>([&](auto t) { rdZ(t); });
  struct Sch2 { int cum[17], endZ[16], endO[16], endF; };
  constexpr Sch2 s2 = [] { Sch2 r{}; int n = 0;
    for (int i = 4; i < 5; ++i) { n += 3; r.endO[i] = n; }
    for (int i = 0; i < 3; ++i) r.endZ[i] = 0;
    for (int i = 3; i < 6; ++i) { n += 5; r.endZ[i] = n; }
    for (int i = 0; i < 16; ++i) { r.cum[i] = n;
      if (i >= 1 && i <= 5) { n += 3; r.endO[i + 4] = n; }
      if (i == 8) for (int k = 10; k < 13; ++k) { n += 3; r.endO[k] = n; }
      if (i >= 9 && i <= 11) { n += 3; r.endO[i + 4] = n; }
      if (i + 6 < 16) { n += 5; r.endZ[i + 6] = n; }
      if (i == 7) { n += 1; r.endF = n; } }
    r.cum[16] = n; return r; }();
  static_for<16>([&](auto i_) { constexpr int i = i_, n = i + 1;
    constexpr int need0 = n < 16 ? s2.endZ[n] : 0, need1 = i >= 4 ? s2.endO[i] : 0, need2 = i == 8 ? s2.endF : 0;
    constexpr int need = need0 > need1 ? (need0 > need2 ? need0 : need2) : (need1 > need2 ? need1 : need2);
    wait_vm_bar<s2.cum[i] - need>();
    if constexpr (i >= 4) asm volatile("" : "+v"(ob[(i - 4) % 5][0]), "+v"(ob[(i - 4) % 5][1]), "+v"(ob[(i - 4) % 5][2]));
    if constexpr (i == 8) { asm volatile("" : "+v"(fv)); flag_spin(fv, fB, rWs, tag); STAMP(10); ldO(IC<10>{}); ldO(IC<11>{}); ldO(IC<12>{}); SB(); }
    if constexpr (i >= 1 && i <= 5) { ldO(IC<i + 4>{}); SB(); }
    if constexpr (i >= 9 && i <= 11) { ldO(IC<i + 4>{}); SB(); }
    static_for<10>([&](auto t_) { constexpr int t = t_, f = 10 * i + t;
      static_for<3>([&](auto u) { a2[t][u] = mfma(af[f % 5], i < 4 ? of[i & 3][u] : ob[(i + 1) % 5][u], i == 0 ? f32x4{0, 0, 0, 0} : a2[t][u]); SB();
        if constexpr (u == 0 && i + 6 < 16 && t < 5) { pieceZ(IC<i + 6>{}, par, (par ? ZU[(i + 6) % NZ][1] : ZU[(i + 6) % NZ][0]) + zq, IC<t>{}); SB(); } });
      if constexpr (f + 5 < 160) { rdZ(IC<f + 5>{}); SB(); } });
    if constexpr (i == 7) flag_ld(fv, fB, rWs);
  });
  pin(a2);
  STAMP(11); CSTAMP(31);
  f32x4 r[5][3];
  wait_vm_bar<63>();
  if (par) static_for<15>([&](auto i) { *(LAS f32x4*)(w * 15360 + i * 1024 + lane * 16) = a2[i / 3][i % 3]; r[i / 3][i % 3] = a2[5 + i / 3][i % 3]; });
  else     static_for<15>([&](auto i) { *(LAS f32x4*)(w * 15360 + i * 1024 + lane * 16) = a2[5 + i / 3][i % 3]; r[i / 3][i % 3] = a2[i / 3][i % 3]; });
  wait_vm_bar<63>();
  static_for<15>([&](auto i) { r[i / 3][i % 3] += *(LAS f32x4*)((w ^ 2u) * 15360 + i * 1024 + lane * 16); });
  STAMP(12);
  const unsigned pbase = WS_P + (cluster * 4 + hp) * 16 * 15360;
  if (w != hg) {
    static_for<15>([&](auto i) { st_sc1<(i & 3) * 1024>(r[i / 3][i % 3], lane * 16, rWs, pbase + (w * 4 + hg) * 15360 + (i >> 2) * 4096); });
    wait_vm<0>();
    if (lane == 0) flag_st(tag, WS_FP + (((cluster * 4 + hp) * 4 + w) * 4 + hg) * 128, rWs);
    STAMP(13);
  } else {
    const unsigned fP = WS_FP + (((cluster * 4 + hp) * 4 + hg) * 4 + ((hg + 1 + lane % 3u) & 3u)) * 128;
    flag_spin(tag + 1, fP, rWs, tag);
    STAMP(13);
    bf16x8 in[2][15];
    auto ldP = [&](auto o) { const unsigned src = (hg + 1 + o) & 3u;
      static_for<15>([&](auto i) { ld_sc1<(i & 3) * 1024>(in[o & 1][i], lane * 16, rWs, pbase + (hg * 4 + src) * 15360 + (i >> 2) * 4096); }); };
    auto addP = [&](auto o) { static_for<15>([&](auto i) { asm volatile("" : "+v"(in[o & 1][i])); r[i / 3][i % 3] += __builtin_bit_cast(f32x4, in[o & 1][i]); }); SB(); };
    ldP(IC<0>{}); ldP(IC<1>{}); wait_vm<15>(); addP(IC<0>{}); ldP(IC<2>{}); wait_vm<15>(); addP(IC<1>{}); wait_vm<0>(); addP(IC<2>{});
    STAMP(14);
    char* o = p.out + ((size_t)(b * 96 + 48 * qh + c) * 1280 + br * 640 + hp * 160 + par * 80 + 4 * g) * 2;
    static_for<5>([&](auto t) { static_for<3>([&](auto u) { u32x2 v = {cvtpk(r[t][u][0], r[t][u][1]), cvtpk(r[t][u][2], r[t][u][3])}; *(u32x2*)(o + u * 16 * 2560 + t * 32) = v; }); });
    if (hg == 0 && hp == 0 && lane == 0) flag_st(tag, WS_EPOCH + cluster * 256, rWs);
    STAMP(15);
  }
  STAMP_OUT;
}

extern "C" void kernel_launch(void* const* d_in, const int* in_sizes, int n_in, void* d_out, int out_size, void* d_ws, size_t ws_size, hipStream_t stream) {
  Params p;
  for (int i = 0; i < 2; ++i) { p.x[i] = (const char*)d_in[5 * i]; p.k[i] = (const char*)d_in[5 * i + 1]; p.v[i] = (const char*)d_in[5 * i + 2]; p.wq[i] = (const char*)d_in[5 * i + 3]; p.wo[i] = (const char*)d_in[5 * i + 4]; }
  p.out = (char*)d_out; p.ws = (char*)d_ws;
  (void)hipFuncSetAttribute((const void*)fused, hipFuncAttributeMaxDynamicSharedMemorySize, LDS_BYTES);
  fused<<<256, 256, LDS_BYTES, stream>>>(p);
}
```

```cpp
#define DIAG_I 0
#define DIAG_J 0
#include <hip/hip_runtime.h>
#include <hip/hip_bf16.h>
#include <utility>

typedef __bf16   bf16x8 __attribute__((ext_vector_type(8)));
typedef __bf16   bf16x4 __attribute__((ext_vector_type(4)));
typedef __bf16   bf16x2 __attribute__((ext_vector_type(2)));
typedef float    f32x4  __attribute__((ext_vector_type(4)));
typedef float    f32x2  __attribute__((ext_vector_type(2)));
typedef unsigned u32x4  __attribute__((ext_vector_type(4)));
typedef unsigned u32x2  __attribute__((ext_vector_type(2)));
typedef unsigned long long u64;
#define LAS __attribute__((address_space(3)))
#define GAS __attribute__((address_space(1)))
#define DI __device__ __forceinline__
typedef __amdgpu_buffer_rsrc_t rsrc_t;

template <int... Is, class F> DI void static_for_impl(std::integer_sequence<int, Is...>, F&& f) { (f(std::integral_constant<int, Is>{}), ...); }
template <int N, class F> DI void static_for(F&& f) { static_for_impl(std::make_integer_sequence<int, N>{}, f); }

constexpr int SQ = 96, DM = 640, NH = 32, DH = 128, HD = 4096;
constexpr unsigned WS_EPOCH = 0;
constexpr unsigned WS_FO    = 4096;
constexpr unsigned WS_FP    = WS_FO + 16 * 64 * 128;
constexpr unsigned WS_O     = 1u << 20;
constexpr unsigned WS_O_BYTES = 2 * 8 * 32 * 2 * 12288;
constexpr unsigned WS_P     = WS_O + WS_O_BYTES;
constexpr unsigned WS_P_BYTES = 16 * 4 * 4 * 4 * 15360;
constexpr unsigned WS_STAMP = 64u << 20;
constexpr unsigned L_WQ = 0;
constexpr unsigned L_X  = 65536;
constexpr unsigned L_KA = 102400, L_VA = 126976, L_SP = 151552, L_KB = 0, L_VB = 24576;
constexpr unsigned L_SYNC = L_SP + 10240;
constexpr unsigned LDS_BYTES = 163840;
#define NT_W 1
#define NT_X 0

DI unsigned rfl(unsigned x) { return __builtin_amdgcn_readfirstlane(x); }
DI unsigned cvtpk(float lo, float hi) { f32x2 v = {lo, hi}; bf16x2 b = __builtin_convertvector(v, bf16x2); return __builtin_bit_cast(unsigned, b); }
DI bf16x8 pack8(const f32x4& a, const f32x4& b) { u32x4 w = {cvtpk(a[0], a[1]), cvtpk(a[2], a[3]), cvtpk(b[0], b[1]), cvtpk(b[2], b[3])}; return __builtin_bit_cast(bf16x8, w); }
DI f32x4 mfma(bf16x8 a, bf16x8 b, f32x4 c) { return __builtin_amdgcn_mfma_f32_16x16x32_bf16(a, b, c, 0, 0, 0); }
DI bf16x4 trrd(unsigned addr) { return __builtin_amdgcn_ds_read_tr16_b64_v4bf16((LAS bf16x4*)addr); }
DI bf16x8 cat(bf16x4 a, bf16x4 b) { return __builtin_shufflevector(a, b, 0, 1, 2, 3, 4, 5, 6, 7); }
template <bool NT> DI void dma(unsigned lds, unsigned voff, rsrc_t r, unsigned soff) {
  if constexpr (NT) asm volatile("s_mov_b32 m0, %0\n\ts_nop 0\n\tbuffer_load_dwordx4 %1, %2, %3 offen nt lds" :: "s"(lds), "v"(voff), "s"(r), "s"(soff) : "memory");
  else              asm volatile("s_mov_b32 m0, %0\n\ts_nop 0\n\tbuffer_load_dwordx4 %1, %2, %3 offen lds" :: "s"(lds), "v"(voff), "s"(r), "s"(soff) : "memory");
}
template <int N> DI void wait_vm() { asm volatile("s_waitcnt vmcnt(%0)" :: "i"(N) : "memory"); }
#define SB() __builtin_amdgcn_sched_barrier(0)
template <int N> DI void wait_vm_bar() { SB(); __builtin_amdgcn_s_waitcnt(0xC07F); asm volatile("s_waitcnt vmcnt(%0)\n\ts_barrier" :: "i"(N > 63 ? 63 : N) : "memory"); SB(); }
template <int OFF> DI void ld_sc1(bf16x8& d, unsigned voff, rsrc_t r, unsigned soff) { asm volatile("buffer_load_dwordx4 %0, %1, %2, %3 offen offset:%4 sc1" : "=v"(d) : "v"(voff), "s"(r), "s"(soff), "i"(OFF) : "memory"); }
template <int OFF, class T> DI void st_sc1(const T& d, unsigned voff, rsrc_t r, unsigned soff) { asm volatile("buffer_store_dwordx4 %0, %1, %2, %3 offen offset:%4 sc1\n\ts_nop 1" :: "v"(d), "v"(voff), "s"(r), "s"(soff), "i"(OFF) : "memory"); }
DI void flag_ld(u64& d, unsigned voff, rsrc_t r) { asm volatile("buffer_load_dwordx2 %0, %1, %2, 0 offen sc1" : "=v"(d) : "v"(voff), "s"(r) : "memory"); }
DI void flag_st(u64 d, unsigned voff, rsrc_t r) { asm volatile("buffer_store_dwordx2 %0, %1, %2, 0 offen sc1" :: "v"(d), "v"(voff), "s"(r) : "memory"); }
DI void flag_spin(u64 v, unsigned voff, rsrc_t r, u64 tag) {
  unsigned cnt = 1u << 20;
  asm volatile("1:\n\tv_cmp_eq_u64 vcc, %[tag], %[v]\n\ts_nop 1\n\ts_cmp_eq_u64 vcc, exec\n\ts_cbranch_scc1 2f\n\ts_sub_u32 %[cnt], %[cnt], 1\n\ts_cbranch_scc1 2f\n\ts_sleep 1\n\t"
               "buffer_load_dwordx2 %[v], %[off], %[r], 0 offen sc1\n\ts_waitcnt vmcnt(0)\n\ts_branch 1b\n2:"
               : [v] "+v"(v), [cnt] "+s"(cnt) : [tag] "s"(tag), [off] "v"(voff), [r] "s"(r) : "vcc", "scc", "memory");
}
template <int N> DI void pair_sync(unsigned par, unsigned mine, unsigned other, unsigned val) {
  unsigned cnt = 1u << 22, t;
  asm volatile("s_cmp_eq_u32 %[par], 0\n\ts_cbranch_scc1 2f\n\ts_waitcnt vmcnt(%[n])\n\tds_write_b32 %[me], %[val]\n"
               "1:\n\tds_read_b32 %[t], %[ot]\n\ts_waitcnt lgkmcnt(0)\n\tv_cmp_ge_u32 vcc, %[t], %[val]\n\ts_nop 1\n\ts_cmp_eq_u64 vcc, exec\n\ts_cbranch_scc1 2f\n\ts_sub_u32 %[cnt], %[cnt], 1\n\ts_cbranch_scc0 1b\n2:"
               : [t] "=&v"(t), [cnt] "+s"(cnt) : [par] "s"(par), [me] "v"(mine), [ot] "v"(other), [val] "v"(val), [n] "i"(N) : "vcc", "scc", "memory");
}
template <int N> using IC = std::integral_constant<int, N>;
template <class T> DI void pin3(T (&a)[3]) { asm volatile("" : "+v"(a[0]), "+v"(a[1]), "+v"(a[2])); }
template <int N, class T> DI void pin(T (&a)[N][3]) { static_for<N>([&](auto i) { pin3(a[i]); }); }
DI float xmax16(float v) { auto r = __builtin_amdgcn_permlane16_swap(__float_as_uint(v), __float_as_uint(v), false, false); return fmaxf(__uint_as_float(r[0]), __uint_as_float(r[1])); }
DI float xmax32(float v) { auto r = __builtin_amdgcn_permlane32_swap(__float_as_uint(v), __float_as_uint(v), false, false); return fmaxf(__uint_as_float(r[0]), __uint_as_float(r[1])); }
DI float xsum16(float v) { auto r = __builtin_amdgcn_permlane16_swap(__float_as_uint(v), __float_as_uint(v), false, false); return __uint_as_float(r[0]) + __uint_as_float(r[1]); }
DI float xsum32(float v) { auto r = __builtin_amdgcn_permlane32_swap(__float_as_uint(v), __float_as_uint(v), false, false); return __uint_as_float(r[0]) + __uint_as_float(r[1]); }
DI rsrc_t mkrsrc(const void* p, unsigned bytes) { return __builtin_amdgcn_make_buffer_rsrc((void*)p, 0, bytes, 0x00020000); }
DI void poll_flags(const char* ws, unsigned off, bool need, u64 tag) {
  for (unsigned spins = 0; spins < (1u << 20); ++spins) {
    u64 v = tag;
    if (need) v = __hip_atomic_load((const GAS u64*)(ws + off), __ATOMIC_RELAXED, __HIP_MEMORY_SCOPE_AGENT);
    if (__all(v == tag)) break;
    __builtin_amdgcn_s_sleep(1);
  }
}
#define STAMP(i) do { if ((i) == DIAG_I || (i) == DIAG_J) { u64 t_; asm volatile("s_memrealtime %0\n\ts_waitcnt lgkmcnt(0)" : "=s"(t_) :: "memory"); if ((i) == DIAG_I) dI_ = (unsigned)t_; if ((i) == DIAG_J) dJ_ = (unsigned)t_; } } while (0)
#define CSTAMP(i)
#define STAMP_DECL unsigned dI_ = 0, dJ_ = 0
#define STAMP_OUT do { if (blockIdx.x == 100 && w == hg) { const unsigned d_ = dJ_ - dI_; \
   const unsigned t0_ = (unsigned)__builtin_amdgcn_s_memrealtime(); while ((unsigned)__builtin_amdgcn_s_memrealtime() - t0_ < 8u * d_) __builtin_amdgcn_s_sleep(1); } } while (0)

struct Params { const char *x[2], *k[2], *v[2], *wq[2], *wo[2]; char* out; char* ws; };

#define ABL_NODMA 0
#define ABL_NOLDS 0
#define ABL_NOBAR 0
#define S_KA 8
#define S_VA 12
__global__ __launch_bounds__(256, 2) void fused(Params p) {
  extern __shared__ __attribute__((aligned(16))) char smem[];
  const unsigned tid = threadIdx.x, lane = tid & 63u, w = rfl(tid >> 6);
  const unsigned id = blockIdx.x, xcd = id & 7u, slot = id >> 3, br = xcd >> 2, hg = xcd & 3u, b = slot >> 2, hp = slot & 3u;
  const unsigned par = w >> 1, qh = w & 1u, hA = 8 * hg + 2 * hp, cluster = br * 8 + b;
  const unsigned g = lane >> 4, c = lane & 15u, q4 = c >> 2, pp = lane & 3u;
  char* ws = p.ws;
  constexpr unsigned L0 = 0;
  STAMP_DECL;
  STAMP(0);
  const u64 tag = *(const u64*)(ws + WS_EPOCH + cluster * 256) + 1;

  const rsrc_t rX  = mkrsrc(p.x[br] + (size_t)b * SQ * DM * 2, SQ * DM * 2);
  const rsrc_t rWq = mkrsrc(p.wq[br] + hA * DH * 2, DM * HD * 2 - hA * DH * 2);
  const rsrc_t rK  = mkrsrc(p.k[br] + ((size_t)b * NH + hA) * SQ * DH * 2, 2 * SQ * DH * 2);
  const rsrc_t rV  = mkrsrc(p.v[br] + ((size_t)b * NH + hA) * SQ * DH * 2, 2 * SQ * DH * 2);
  const rsrc_t rWo = mkrsrc(p.wo[br] + (size_t)hg * 1024 * DM * 2 + hp * 320, 1024 * DM * 2 - hp * 320);
  const rsrc_t rWs = mkrsrc(ws, WS_P + WS_P_BYTES);

  const unsigned hi = lane >> 5, c32 = lane & 31u;
  unsigned vWq[2];
  { const unsigned r3 = (2 * w + hi) & 3u;
    vWq[0] = (2 * w + hi) * 8192 + ((c32 ^ (r3 << 1)) << 4);
    vWq[1] = (2 * w + hi) * 8192 + ((c32 ^ ((r3 | 4u) << 1)) << 4); }
  const unsigned vX = (8 * w + (lane >> 3)) * 1280 + (((lane & 7u) ^ ((4 * (w & 1u) + (lane >> 4)) & 7u)) << 4);
  const unsigned vK = (4 * w + g) * 256 + ((c ^ ((4 * w + g) & 15u)) << 4);
  const unsigned vV = (4 * w + g) * 256 + ((c ^ (((4 * w + g) & 7u) << 1)) << 4);
  const unsigned wk = w * 1024;
  unsigned vKB[2];
  vKB[0] = (4 * qh + g) * 256 + ((c ^ (4 * qh + g)) << 4); vKB[1] = (4 * qh + 8 + g) * 256 + ((c ^ (4 * qh + 8 + g)) << 4);
  const unsigned vVB = (4 * qh + g) * 256 + ((c ^ (((4 * qh + g) & 7u) << 1)) << 4);
  if (tid < 4) *(LAS unsigned*)(L_SYNC + tid * 4) = 0;

  auto pieceU = [&](auto s_, auto j) { constexpr int s = s_; dma<NT_W>(wk + (L_WQ + (s & 3) * 16384 + 4096 * j), vWq[j & 1], rWq, (unsigned)(32 * s + 8 * j) * 8192); };
  auto pieceE = [&](auto e_, auto j) { constexpr int e = e_; dma<NT_X>(wk + (L_X + (e % 3) * 12288 + 4096 * j), vX, rX, (unsigned)(32 * j * 1280 + 128 * e)); };
  auto issueU = [&](auto s_) { static_for<4>([&](auto j) { pieceU(s_, j); }); };
  auto issueE = [&](auto e_) { static_for<3>([&](auto j) { pieceE(e_, j); }); };

  const unsigned F = q4 | ((g & 1u) << 2);
  unsigned aWq[8];
  static_for<8>([&](auto t) { aWq[t] = (L_WQ + (8 * g + q4) * 512 + par * 256 + F * 32 + 8 * pp) ^ (t << 5); });
  unsigned aX[2];
  aX[0] = L_X + (48 * qh + c) * 128 + ((g ^ ((c >> 1) & 7u)) << 4); aX[1] = aX[0] ^ 64u;

  issueU(IC<0>{}); issueE(IC<0>{}); issueU(IC<1>{}); issueE(IC<1>{}); issueU(IC<2>{}); issueU(IC<3>{});
  f32x4 acc[8][3];
  static_for<8>([&](auto t) { static_for<3>([&](auto u) { acc[t][u] = f32x4{0, 0, 0, 0}; }); });
  bf16x8 fa[8], fb[2][3];
  struct Sch { int cum[22]; int endU[21]; };
  constexpr Sch sch = [] { Sch r{}; int n = 0;
    n += 7; r.endU[0] = n; n += 4; r.endU[1] = n; n += 7; r.endU[2] = n; n += 4; r.endU[3] = n;
    for (int s = 0; s <= 20; ++s) { r.cum[s] = n;
      if (s + 4 < 20) { if ((s & 1) == 0) n += 3; n += 4; r.endU[s + 4] = n; }
      if (s == S_KA || s == S_VA) n += 6; }
    r.cum[21] = n; return r; }();
  wait_vm_bar<sch.cum[0] - sch.endU[0]>();
  STAMP(1); CSTAMP(28);
  static_for<3>([&](auto u) { fb[0][u] = *(LAS bf16x8*)(aX[0] + u * 2048); if (ABL_NOLDS) fb[1][u] = *(LAS bf16x8*)(aX[1] + u * 2048); });
  static_for<8>([&](auto t) { fa[t] = cat(trrd(aWq[t]), trrd(aWq[t] + 2048)); });
  static_for<20>([&](auto s_) { constexpr int s = s_, n = s + 1;
    if constexpr (s == 18) { if (par) wait_vm_bar<sch.cum[s] - sch.endU[n] + 12>(); else wait_vm_bar<sch.cum[s] - sch.endU[n]>(); }
    else if constexpr (n < 20 && !ABL_NOBAR) wait_vm_bar<sch.cum[s] - sch.endU[n]>();
    static_for<8>([&](auto t_) { constexpr int t = t_;
      acc[t][0] = mfma(fa[t], fb[s & 1][0], acc[t][0]); SB();
      if constexpr (n < 20 && t < 3 && !ABL_NOLDS) { fb[n & 1][t] = *(LAS bf16x8*)(aX[n & 1] + ((n >> 1) % 3) * 12288 + t * 2048); SB(); }
      if constexpr (s + 4 < 20 && !ABL_NODMA) {
        if constexpr ((s & 1) == 0) { if constexpr (t < 3) pieceE(IC<(s + 4) / 2>{}, IC<t>{}); else if constexpr (t < 7) pieceU(IC<s + 4>{}, IC<t - 3>{}); }
        else if constexpr (t < 4) pieceU(IC<s + 4>{}, IC<t>{});
        SB(); }
      acc[t][1] = mfma(fa[t], fb[s & 1][1], acc[t][1]); SB();
      acc[t][2] = mfma(fa[t], fb[s & 1][2], acc[t][2]); SB();
      if constexpr (n < 20 && !ABL_NOLDS) { const unsigned a = aWq[t] + (n & 3) * 16384; fa[t] = cat(trrd(a), trrd(a + 2048)); SB(); }
    });
    if constexpr (s == S_KA) static_for<6>([&](auto j) { dma<NT_W>(wk + (L_KA + 4096 * j), vK, rK, j * 4096); });
    if constexpr (s == S_VA) static_for<6>([&](auto j) { dma<NT_W>(wk + (L_VA + 4096 * j), vV, rV, j * 4096); });
    if constexpr (s == 17) { if (par) static_for<12>([&](auto j) { dma<NT_W>(qh * 1024 + (L_KB + 2048 * j), vKB[j & 1], rK, 24576 + (j >> 1) * 4096); }); }
    if constexpr (s == 18) { if (par) static_for<12>([&](auto j) { dma<NT_W>(qh * 1024 + (L_VB + 2048 * j), vVB, rV, 24576 + j * 2048); }); }
  });
  pin(acc);
  STAMP(2); CSTAMP(29);
  constexpr int NZ = 7;
  constexpr unsigned ZU[NZ][2] = {{49152, 59392}, {69632, 79872}, {90112, L_SP}, {L_KA, L_KA + 10240}, {L_KA + 20480, L_KA + 30720}, {0, 10240}, {20480, 30720}};
  unsigned vWo[5];
  static_for<5>([&](auto i) { const unsigned e = i * 64 + lane, row = e / 20, pos = e - row * 20; vWo[i] = (row + 16 * qh) * 1280 + ((pos ^ (((row >> 2) & 1u) << 1)) << 4); });
  auto p2_row = [&](int i, unsigned pr) -> unsigned {
    if (i < 4) return (2 * hp + pr) * 4 + i; const unsigned a = (i - 4) % 6 + 6 * pr; return (2 * ((hp + 1 + (a >> 2)) & 3u) + (i >= 10 ? 1u : 0u)) * 4 + (a & 3u); };
  auto pieceZ = [&](auto i_, unsigned pr, unsigned ld, auto j) { constexpr int i = i_; dma<NT_W>(ld + j * 1024, vWo[j], rWo, p2_row(i, pr) * (32 * 1280)); };
  wait_vm_bar<63>();
  if (par) static_for<3>([&](auto i) { static_for<2>([&](auto pr) { static_for<5>([&](auto j) { pieceZ(i, pr, ZU[i][pr] + qh * 5120, j); }); }); });

  bf16x8 qf[4][3];
  static_for<4>([&](auto s) { static_for<3>([&](auto u) { qf[s][u] = pack8(acc[2 * s][u], acc[2 * s + 1][u]); }); });
  SB(); pair_sync<42>(par, L_SYNC + w * 4, L_SYNC + (w ^ 1u) * 4, 1); SB();
  STAMP(3);
  f32x4 st[6][3];
  { const unsigned aK0 = (par ? L_KB : L_KA) + c * 256 + (((g >> 1) ^ c) << 4) + 8 * (g & 1u);
    bf16x8 kf[4];
    auto rdK = [&](auto i_) { constexpr int i = i_, kt = i >> 2, s = i & 3;
      kf[s] = cat(*(LAS bf16x4*)((aK0 ^ (64 * s)) + kt * 4096), *(LAS bf16x4*)((aK0 ^ (64 * s + 32)) + kt * 4096)); };
    static_for<4>([&](auto i) { rdK(i); });
    SB();
    static_for<24>([&](auto i_) { constexpr int i = i_, kt = i >> 2, s = i & 3;
      static_for<3>([&](auto u) { st[kt][u] = mfma(kf[s], qf[s][u], s == 0 ? f32x4{0, 0, 0, 0} : st[kt][u]); });
      SB();
      if constexpr (i + 4 < 24) { rdK(IC<i + 4>{}); SB(); } }); }
  bf16x8 pf[3][3];
  constexpr float CS = 0.08838834764831845f * 1.4426950408889634f;
  static_for<3>([&](auto u) {
    float m = st[0][u][0];
    static_for<6>([&](auto kt) { static_for<4>([&](auto r) { m = fmaxf(m, st[kt][u][(int)r]); }); });
    m = xmax32(xmax16(m));
    const float mc = -m * CS; float l = 0.f;
    static_for<6>([&](auto kt) { static_for<4>([&](auto r) { const float e = __builtin_amdgcn_exp2f(fmaf(st[kt][u][(int)r], CS, mc)); st[kt][u][(int)r] = e; l += e; }); });
    l = xsum32(xsum16(l));
    const float il = __builtin_amdgcn_rcpf(l);
    static_for<3>([&](auto s) { pf[s][u] = pack8(st[2 * s][u] * il, st[2 * s + 1][u] * il); });
  });
  STAMP(4);
  SB(); pair_sync<30>(par, L_SYNC + w * 4, L_SYNC + (w ^ 1u) * 4, 2); SB();
  STAMP(5);
  const unsigned fA = WS_FO + (((cluster * 4 + hg) * 4 + ((hp + 1 + ((6 * par + (lane & 1u) * 5) >> 2)) & 3u)) * 4 + qh) * 128, fB = fA + 256;
  bf16x8 of[4][3];
  { f32x4 ot[8][3];
    const unsigned aV0 = (par ? L_VB : L_VA) + (4 * g + q4) * 256 + F * 32 + 8 * pp;
    bf16x8 vf[4];
    auto rdV = [&](auto i_) { constexpr int i = i_, t = i / 3, s = i % 3; const unsigned a = (aV0 ^ (t << 5)) + s * 8192; vf[i & 3] = cat(trrd(a), trrd(a + 4096)); };
    static_for<4>([&](auto i) { rdV(i); });
    SB();
    const unsigned ob = WS_O + ((cluster * 32 + hA + par) * 2 + qh) * 12288;
    static_for<24>([&](auto i_) { constexpr int i = i_, t = i / 3, s = i % 3;
      static_for<3>([&](auto u) { ot[t][u] = mfma(vf[i & 3], pf[s][u], s == 0 ? f32x4{0, 0, 0, 0} : ot[t][u]); });
      SB();
      if constexpr (i + 4 < 24) { rdV(IC<i + 4>{}); SB(); }
      if constexpr (i >= 8 && (i - 8) % 6 < 3) { constexpr int k = (i - 8) / 6, u = (i - 8) % 6;
        of[k][u] = pack8(ot[2 * k][u], ot[2 * k + 1][u]); st_sc1<u * 1024>(of[k][u], lane * 16, rWs, ob + k * 3072); SB(); } });
    static_for<3>([&](auto u) { of[3][u] = pack8(ot[6][u], ot[7][u]); st_sc1<u * 1024>(of[3][u], lane * 16, rWs, ob + 3 * 3072); });
  }
  STAMP(6);
  u64 fv;
  flag_ld(fv, fA, rWs);
  wait_vm<1>();
  if (lane == 0) flag_st(tag, WS_FO + (((cluster * 4 + hg) * 4 + hp) * 4 + w) * 128, rWs);
  STAMP(7);
  asm volatile("s_waitcnt vmcnt(1)" : "+v"(fv) :: "memory");
  flag_spin(fv, fA, rWs, tag);
  STAMP(8);
  bf16x8 ob[5][3];
  auto ldO = [&](auto i_) { constexpr int i = i_; const unsigned rw = p2_row(i, par), so = WS_O + ((cluster * 32 + 8 * hg + (rw >> 2)) * 2 + qh) * 12288 + (rw & 3u) * 3072;
    static_for<3>([&](auto u) { ld_sc1<u * 1024>(ob[(i - 4) % 5][u], lane * 16, rWs, so); }); };
  ldO(IC<4>{});
  wait_vm_bar<3>();
  STAMP(9); CSTAMP(30);
  const unsigned zq = qh * 5120;
  static_for<3>([&](auto k) { static_for<5>([&](auto j) { pieceZ(IC<3 + k>{}, par, (par ? ZU[3 + k][1] : ZU[3 + k][0]) + zq, j); }); });
  f32x4 a2[10][3];
  const unsigned aZe = (4 * g + q4) * 320 + (g & 1u) * 32 + 8 * pp, aZo = (4 * g + q4) * 320 - (g & 1u) * 32 + 8 * pp;
  bf16x8 af[5];
  auto rdZ = [&](auto f_) { constexpr int f = f_, i = f / 10, t = f % 10; const unsigned a = ((t & 1) ? aZo : aZe) + (par ? ZU[i % NZ][1] : ZU[i % NZ][0]) + t * 32; af[f % 5] = cat(trrd(a), trrd(a + 5120)); };
  static_for<5>([&](auto t) { rdZ(t); });
  struct Sch2 { int cum[17], endZ[16], endO[16], endF; };
  constexpr Sch2 s2 = [] { Sch2 r{}; int n = 0;
    for (int i = 4; i < 5; ++i) { n += 3; r.endO[i] = n; }
    for (int i = 0; i < 3; ++i) r.endZ[i] = 0;
    for (int i = 3; i < 6; ++i) { n += 5; r.endZ[i] = n; }
    for (int i = 0; i < 16; ++i) { r.cum[i] = n;
      if (i >= 1 && i <= 5) { n += 3; r.endO[i + 4] = n; }
      if (i == 8) for (int k = 10; k < 13; ++k) { n += 3; r.endO[k] = n; }
      if (i >= 9 && i <= 11) { n += 3; r.endO[i + 4] = n; }
      if (i + 6 < 16) { n += 5; r.endZ[i + 6] = n; }
      if (i == 7) { n += 1; r.endF = n; } }
    r.cum[16] = n; return r; }();
  static_for<16>([&](auto i_) { constexpr int i = i_, n = i + 1;
    constexpr int need0 = n < 16 ? s2.endZ[n] : 0, need1 = i >= 4 ? s2.endO[i] : 0, need2 = i == 8 ? s2.endF : 0;
    constexpr int need = need0 > need1 ? (need0 > need2 ? need0 : need2) : (need1 > need2 ? need1 : need2);
    wait_vm_bar<s2.cum[i] - need>();
    if constexpr (i >= 4) asm volatile("" : "+v"(ob[(i - 4) % 5][0]), "+v"(ob[(i - 4) % 5][1]), "+v"(ob[(i - 4) % 5][2]));
    if constexpr (i == 8) { asm volatile("" : "+v"(fv)); flag_spin(fv, fB, rWs, tag); STAMP(10); ldO(IC<10>{}); ldO(IC<11>{}); ldO(IC<12>{}); SB(); }
    if constexpr (i >= 1 && i <= 5) { ldO(IC<i + 4>{}); SB(); }
    if constexpr (i >= 9 && i <= 11) { ldO(IC<i + 4>{}); SB(); }
    static_for<10>([&](auto t_) { constexpr int t = t_, f = 10 * i + t;
      static_for<3>([&](auto u) { a2[t][u] = mfma(af[f % 5], i < 4 ? of[i & 3][u] : ob[(i + 1) % 5][u], i == 0 ? f32x4{0, 0, 0, 0} : a2[t][u]); SB();
        if constexpr (u == 0 && i + 6 < 16 && t < 5) { pieceZ(IC<i + 6>{}, par, (par ? ZU[(i + 6) % NZ][1] : ZU[(i + 6) % NZ][0]) + zq, IC<t>{}); SB(); } });
      if constexpr (f + 5 < 160) { rdZ(IC<f + 5>{}); SB(); } });
    if constexpr (i == 7) flag_ld(fv, fB, rWs);
  });
  pin(a2);
  STAMP(11); CSTAMP(31);
  f32x4 r[5][3];
  wait_vm_bar<63>();
  if (par) static_for<15>([&](auto i) { *(LAS f32x4*)(w * 15360 + i * 1024 + lane * 16) = a2[i / 3][i % 3]; r[i / 3][i % 3] = a2[5 + i / 3][i % 3]; });
  else     static_for<15>([&](auto i) { *(LAS f32x4*)(w * 15360 + i * 1024 + lane * 16) = a2[5 + i / 3][i % 3]; r[i / 3][i % 3] = a2[i / 3][i % 3]; });
  wait_vm_bar<63>();
  static_for<15>([&](auto i) { r[i / 3][i % 3] += *(LAS f32x4*)((w ^ 2u) * 15360 + i * 1024 + lane * 16); });
  STAMP(12);
  const unsigned pbase = WS_P + (cluster * 4 + hp) * 16 * 15360;
  if (w != hg) {
    static_for<15>([&](auto i) { st_sc1<(i & 3) * 1024>(r[i / 3][i % 3], lane * 16, rWs, pbase + (w * 4 + hg) * 15360 + (i >> 2) * 4096); });
    wait_vm<0>();
    if (lane == 0) flag_st(tag, WS_FP + (((cluster * 4 + hp) * 4 + w) * 4 + hg) * 128, rWs);
    STAMP(13);
  } else {
    const unsigned fP = WS_FP + (((cluster * 4 + hp) * 4 + hg) * 4 + ((hg + 1 + lane % 3u) & 3u)) * 128;
    flag_spin(tag + 1, fP, rWs, tag);
    STAMP(13);
    bf16x8 in[2][15];
    auto ldP = [&](auto o) { const unsigned src = (hg + 1 + o) & 3u;
      static_for<15>([&](auto i) { ld_sc1<(i & 3) * 1024>(in[o & 1][i], lane * 16, rWs, pbase + (hg * 4 + src) * 15360 + (i >> 2) * 4096); }); };
    auto addP = [&](auto o) { static_for<15>([&](auto i) { asm volatile("" : "+v"(in[o & 1][i])); r[i / 3][i % 3] += __builtin_bit_cast(f32x4, in[o & 1][i]); }); SB(); };
    ldP(IC<0>{}); ldP(IC<1>{}); wait_vm<15>(); addP(IC<0>{}); ldP(IC<2>{}); wait_vm<15>(); addP(IC<1>{}); wait_vm<0>(); addP(IC<2>{});
    STAMP(14);
    char* o = p.out + ((size_t)(b * 96 + 48 * qh + c) * 1280 + br * 640 + hp * 160 + par * 80 + 4 * g) * 2;
    static_for<5>([&](auto t) { static_for<3>([&](auto u) { u32x2 v = {cvtpk(r[t][u][0], r[t][u][1]), cvtpk(r[t][u][2], r[t][u][3])}; *(u32x2*)(o + u * 16 * 2560 + t * 32) = v; }); });
    if (hg == 0 && hp == 0 && lane == 0) flag_st(tag, WS_EPOCH + cluster * 256, rWs);
    STAMP(15);
  }
  STAMP_OUT;
}

extern "C" void kernel_launch(void* const* d_in, const int* in_sizes, int n_in, void* d_out, int out_size, void* d_ws, size_t ws_size, hipStream_t stream) {
  Params p;
  for (int i = 0; i < 2; ++i) { p.x[i] = (const char*)d_in[5 * i]; p.k[i] = (const char*)d_in[5 * i + 1]; p.v[i] = (const char*)d_in[5 * i + 2]; p.wq[i] = (const char*)d_in[5 * i + 3]; p.wo[i] = (const char*)d_in[5 * i + 4]; }
  p.out = (char*)d_out; p.ws = (char*)d_ws;
  (void)hipFuncSetAttribute((const void*)fused, hipFuncAttributeMaxDynamicSharedMemorySize, LDS_BYTES);
  fused<<<256, 256, LDS_BYTES, stream>>>(p);
}
```

```cpp
#define DIAG_I 0
#define DIAG_J 0
#include <hip/hip_runtime.h>
#include <hip/hip_bf16.h>
#include <utility>

typedef __bf16   bf16x8 __attribute__((ext_vector_type(8)));
typedef __bf16   bf16x4 __attribute__((ext_vector_type(4)));
typedef __bf16   bf16x2 __attribute__((ext_vector_type(2)));
typedef float    f32x4  __attribute__((ext_vector_type(4)));
typedef float    f32x2  __attribute__((ext_vector_type(2)));
typedef unsigned u32x4  __attribute__((ext_vector_type(4)));
typedef unsigned u32x2  __attribute__((ext_vector_type(2)));
typedef unsigned long long u64;
#define LAS __attribute__((address_space(3)))
#define GAS __attribute__((address_space(1)))
#define DI __device__ __forceinline__
typedef __amdgpu_buffer_rsrc_t rsrc_t;

template <int... Is, class F> DI void static_for_impl(std::integer_sequence<int, Is...>, F&& f) { (f(std::integral_constant<int, Is>{}), ...); }
template <int N, class F> DI void static_for(F&& f) { static_for_impl(std::make_integer_sequence<int, N>{}, f); }

constexpr int SQ = 96, DM = 640, NH = 32, DH = 128, HD = 4096;
constexpr unsigned WS_EPOCH = 0;
constexpr unsigned WS_FO    = 4096;
constexpr unsigned WS_FP    = WS_FO + 16 * 64 * 128;
constexpr unsigned WS_O     = 1u << 20;
constexpr unsigned WS_O_BYTES = 2 * 8 * 32 * 2 * 12288;
constexpr unsigned WS_P     = WS_O + WS_O_BYTES;
constexpr unsigned WS_P_BYTES = 16 * 4 * 4 * 4 * 15360;
constexpr unsigned WS_STAMP = 64u << 20;
constexpr unsigned L_WQ = 0;
constexpr unsigned L_X  = 65536;
constexpr unsigned L_KA = 102400, L_VA = 126976, L_SP = 151552, L_KB = 0, L_VB = 24576;
constexpr unsigned L_SYNC = L_SP + 10240;
constexpr unsigned LDS_BYTES = 163840;
#define NT_W 1
#define NT_X 0

DI unsigned rfl(unsigned x) { return __builtin_amdgcn_readfirstlane(x); }
DI unsigned cvtpk(float lo, float hi) { f32x2 v = {lo, hi}; bf16x2 b = __builtin_convertvector(v, bf16x2); return __builtin_bit_cast(unsigned, b); }
DI bf16x8 pack8(const f32x4& a, const f32x4& b) { u32x4 w = {cvtpk(a[0], a[1]), cvtpk(a[2], a[3]), cvtpk(b[0], b[1]), cvtpk(b[2], b[3])}; return __builtin_bit_cast(bf16x8, w); }
DI f32x4 mfma(bf16x8 a, bf16x8 b, f32x4 c) { return __builtin_amdgcn_mfma_f32_16x16x32_bf16(a, b, c, 0, 0, 0); }
DI bf16x4 trrd(unsigned addr) { return __builtin_amdgcn_ds_read_tr16_b64_v4bf16((LAS bf16x4*)addr); }
DI bf16x8 cat(bf16x4 a, bf16x4 b) { return __builtin_shufflevector(a, b, 0, 1, 2, 3, 4, 5, 6, 7); }
template <bool NT> DI void dma(unsigned lds, unsigned voff, rsrc_t r, unsigned soff) {
  if constexpr (NT) asm volatile("s_mov_b32 m0, %0\n\ts_nop 0\n\tbuffer_load_dwordx4 %1, %2, %3 offen nt lds" :: "s"(lds), "v"(voff), "s"(r), "s"(soff) : "memory");
  else              asm volatile("s_mov_b32 m0, %0\n\ts_nop 0\n\tbuffer_load_dwordx4 %1, %2, %3 offen lds" :: "s"(lds), "v"(voff), "s"(r), "s"(soff) : "memory");
}
template <int N> DI void wait_vm() { asm volatile("s_waitcnt vmcnt(%0)" :: "i"(N) : "memory"); }
#define SB() __builtin_amdgcn_sched_barrier(0)
template <int N> DI void wait_vm_bar() { SB(); __builtin_amdgcn_s_waitcnt(0xC07F); asm volatile("s_waitcnt vmcnt(%0)\n\ts_barrier" :: "i"(N > 63 ? 63 : N) : "memory"); SB(); }
template <int OFF> DI void ld_sc1(bf16x8& d, unsigned voff, rsrc_t r, unsigned soff) { asm volatile("buffer_load_dwordx4 %0, %1, %2, %3 offen offset:%4 sc1" : "=v"(d) : "v"(voff), "s"(r), "s"(soff), "i"(OFF) : "memory"); }
template <int OFF, class T> DI void st_sc1(const T& d, unsigned voff, rsrc_t r, unsigned soff) { asm volatile("buffer_store_dwordx4 %0, %1, %2, %3 offen offset:%4 sc1\n\ts_nop 1" :: "v"(d), "v"(voff), "s"(r), "s"(soff), "i"(OFF) : "memory"); }
DI void flag_ld(u64& d, unsigned voff, rsrc_t r) { asm volatile("buffer_load_dwordx2 %0, %1, %2, 0 offen sc1" : "=v"(d) : "v"(voff), "s"(r) : "memory"); }
DI void flag_st(u64 d, unsigned voff, rsrc_t r) { asm volatile("buffer_store_dwordx2 %0, %1, %2, 0 offen sc1" :: "v"(d), "v"(voff), "s"(r) : "memory"); }
DI void flag_spin(u64 v, unsigned voff, rsrc_t r, u64 tag) {
  unsigned cnt = 1u << 20;
  asm volatile("1:\n\tv_cmp_eq_u64 vcc, %[tag], %[v]\n\ts_nop 1\n\ts_cmp_eq_u64 vcc, exec\n\ts_cbranch_scc1 2f\n\ts_sub_u32 %[cnt], %[cnt], 1\n\ts_cbranch_scc1 2f\n\ts_sleep 1\n\t"
               "buffer_load_dwordx2 %[v], %[off], %[r], 0 offen sc1\n\ts_waitcnt vmcnt(0)\n\ts_branch 1b\n2:"
               : [v] "+v"(v), [cnt] "+s"(cnt) : [tag] "s"(tag), [off] "v"(voff), [r] "s"(r) : "vcc", "scc", "memory");
}
template <int N> DI void pair_sync(unsigned par, unsigned mine, unsigned other, unsigned val) {
  unsigned cnt = 1u << 22, t;
  asm volatile("s_cmp_eq_u32 %[par], 0\n\ts_cbranch_scc1 2f\n\ts_waitcnt vmcnt(%[n])\n\tds_write_b32 %[me], %[val]\n"
               "1:\n\tds_read_b32 %[t], %[ot]\n\ts_waitcnt lgkmcnt(0)\n\tv_cmp_ge_u32 vcc, %[t], %[val]\n\ts_nop 1\n\ts_cmp_eq_u64 vcc, exec\n\ts_cbranch_scc1 2f\n\ts_sub_u32 %[cnt], %[cnt], 1\n\ts_cbranch_scc0 1b\n2:"
               : [t] "=&v"(t), [cnt] "+s"(cnt) : [par] "s"(par), [me] "v"(mine), [ot] "v"(other), [val] "v"(val), [n] "i"(N) : "vcc", "scc", "memory");
}
template <int N> using IC = std::integral_constant<int, N>;
template <class T> DI void pin3(T (&a)[3]) { asm volatile("" : "+v"(a[0]), "+v"(a[1]), "+v"(a[2])); }
template <int N, class T> DI void pin(T (&a)[N][3]) { static_for<N>([&](auto i) { pin3(a[i]); }); }
DI float xmax16(float v) { auto r = __builtin_amdgcn_permlane16_swap(__float_as_uint(v), __float_as_uint(v), false, false); return fmaxf(__uint_as_float(r[0]), __uint_as_float(r[1])); }
DI float xmax32(float v) { auto r = __builtin_amdgcn_permlane32_swap(__float_as_uint(v), __float_as_uint(v), false, false); return fmaxf(__uint_as_float(r[0]), __uint_as_float(r[1])); }
DI float xsum16(float v) { auto r = __builtin_amdgcn_permlane16_swap(__float_as_uint(v), __float_as_uint(v), false, false); return __uint_as_float(r[0]) + __uint_as_float(r[1]); }
DI float xsum32(float v) { auto r = __builtin_amdgcn_permlane32_swap(__float_as_uint(v), __float_as_uint(v), false, false); return __uint_as_float(r[0]) + __uint_as_float(r[1]); }
DI rsrc_t mkrsrc(const void* p, unsigned bytes) { return __builtin_amdgcn_make_buffer_rsrc((void*)p, 0, bytes, 0x00020000); }
DI void poll_flags(const char* ws, unsigned off, bool need, u64 tag) {
  for (unsigned spins = 0; spins < (1u << 20); ++spins) {
    u64 v = tag;
    if (need) v = __hip_atomic_load((const GAS u64*)(ws + off), __ATOMIC_RELAXED, __HIP_MEMORY_SCOPE_AGENT);
    if (__all(v == tag)) break;
    __builtin_amdgcn_s_sleep(1);
  }
}
#define STAMP(i) do { if ((i) == DIAG_I || (i) == DIAG_J) { u64 t_; asm volatile("s_memrealtime %0\n\ts_waitcnt lgkmcnt(0)" : "=s"(t_) :: "memory"); if ((i) == DIAG_I) dI_ = (unsigned)t_; if ((i) == DIAG_J) dJ_ = (unsigned)t_; } } while (0)
#define CSTAMP(i)
#define STAMP_DECL unsigned dI_ = 0, dJ_ = 0
#define STAMP_OUT do { if (blockIdx.x == 100 && w == hg) { const unsigned d_ = dJ_ - dI_; \
   const unsigned t0_ = (unsigned)__builtin_amdgcn_s_memrealtime(); while ((unsigned)__builtin_amdgcn_s_memrealtime() - t0_ < 8u * d_) __builtin_amdgcn_s_sleep(1); } } while (0)

__global__ void postk(char* ws, int what) {
  __shared__ unsigned st[256], en[256];
  st[threadIdx.x] = *(volatile unsigned*)(ws + WS_STAMP + threadIdx.x * 8); en[threadIdx.x] = *(volatile unsigned*)(ws + WS_STAMP + threadIdx.x * 8 + 4);
  __syncthreads();
  if (threadIdx.x == 0) { const unsigned ref = st[0]; int mns = 0, mxs = 0, mxe = 0;
    for (int i = 0; i < 256; ++i) { int a = (int)(st[i] - ref), b = (int)(en[i] - ref); mns = min(mns, a); mxs = max(mxs, a); mxe = max(mxe, b); }
    unsigned d = what == 1 ? mxe - mns : what == 2 ? mxs - mns : what == 3 ? mxe - (int)(en[100] - ref) : 0;
    const unsigned t1 = (unsigned)__builtin_amdgcn_s_memrealtime(); while ((unsigned)__builtin_amdgcn_s_memrealtime() - t1 < 8 * d) __builtin_amdgcn_s_sleep(1); } }
struct Params { const char *x[2], *k[2], *v[2], *wq[2], *wo[2]; char* out; char* ws; };

#define ABL_NODMA 0
#define ABL_NOLDS 0
#define ABL_NOBAR 0
#define S_KA 8
#define S_VA 12
__global__ __launch_bounds__(256, 2) void fused(Params p) {
  extern __shared__ __attribute__((aligned(16))) char smem[];
  const unsigned tid = threadIdx.x, lane = tid & 63u, w = rfl(tid >> 6);
  const unsigned id = blockIdx.x, xcd = id & 7u, slot = id >> 3, br = xcd >> 2, hg = xcd & 3u, b = slot >> 2, hp = slot & 3u;
  const unsigned par = w >> 1, qh = w & 1u, hA = 8 * hg + 2 * hp, cluster = br * 8 + b;
  const unsigned g = lane >> 4, c = lane & 15u, q4 = c >> 2, pp = lane & 3u;
  char* ws = p.ws;
  constexpr unsigned L0 = 0;
  STAMP_DECL;
  STAMP(0);
  const u64 tag = *(const u64*)(ws + WS_EPOCH + cluster * 256) + 1;

  const rsrc_t rX  = mkrsrc(p.x[br] + (size_t)b * SQ * DM * 2, SQ * DM * 2);
  const rsrc_t rWq = mkrsrc(p.wq[br] + hA * DH * 2, DM * HD * 2 - hA * DH * 2);
  const rsrc_t rK  = mkrsrc(p.k[br] + ((size_t)b * NH + hA) * SQ * DH * 2, 2 * SQ * DH * 2);
  const rsrc_t rV  = mkrsrc(p.v[br] + ((size_t)b * NH + hA) * SQ * DH * 2, 2 * SQ * DH * 2);
  const rsrc_t rWo = mkrsrc(p.wo[br] + (size_t)hg * 1024 * DM * 2 + hp * 320, 1024 * DM * 2 - hp * 320);
  const rsrc_t rWs = mkrsrc(ws, WS_P + WS_P_BYTES);

  const unsigned hi = lane >> 5, c32 = lane & 31u;
  unsigned vWq[2];
  { const unsigned r3 = (2 * w + hi) & 3u;
    vWq[0] = (2 * w + hi) * 8192 + ((c32 ^ (r3 << 1)) << 4);
    vWq[1] = (2 * w + hi) * 8192 + ((c32 ^ ((r3 | 4u) << 1)) << 4); }
  const unsigned vX = (8 * w + (lane >> 3)) * 1280 + (((lane & 7u) ^ ((4 * (w & 1u) + (lane >> 4)) & 7u)) << 4);
  const unsigned vK = (4 * w + g) * 256 + ((c ^ ((4 * w + g) & 15u)) << 4);
  const unsigned vV = (4 * w + g) * 256 + ((c ^ (((4 * w + g) & 7u) << 1)) << 4);
  const unsigned wk = w * 1024;
  unsigned vKB[2];
  vKB[0] = (4 * qh + g) * 256 + ((c ^ (4 * qh + g)) << 4); vKB[1] = (4 * qh + 8 + g) * 256 + ((c ^ (4 * qh + 8 + g)) << 4);
  const unsigned vVB = (4 * qh + g) * 256 + ((c ^ (((4 * qh + g) & 7u) << 1)) << 4);
  if (tid < 4) *(LAS unsigned*)(L_SYNC + tid * 4) = 0;

  auto pieceU = [&](auto s_, auto j) { constexpr int s = s_; dma<NT_W>(wk + (L_WQ + (s & 3) * 16384 + 4096 * j), vWq[j & 1], rWq, (unsigned)(32 * s + 8 * j) * 8192); };
  auto pieceE = [&](auto e_, auto j) { constexpr int e = e_; dma<NT_X>(wk + (L_X + (e % 3) * 12288 + 4096 * j), vX, rX, (unsigned)(32 * j * 1280 + 128 * e)); };
  auto issueU = [&](auto s_) { static_for<4>([&](auto j) { pieceU(s_, j); }); };
  auto issueE = [&](auto e_) { static_for<3>([&](auto j) { pieceE(e_, j); }); };

  const unsigned F = q4 | ((g & 1u) << 2);
  unsigned aWq[8];
  static_for<8>([&](auto t) { aWq[t] = (L_WQ + (8 * g + q4) * 512 + par * 256 + F * 32 + 8 * pp) ^ (t << 5); });
  unsigned aX[2];
  aX[0] = L_X + (48 * qh + c) * 128 + ((g ^ ((c >> 1) & 7u)) << 4); aX[1] = aX[0] ^ 64u;

  issueU(IC<0>{}); issueE(IC<0>{}); issueU(IC<1>{}); issueE(IC<1>{}); issueU(IC<2>{}); issueU(IC<3>{});
  f32x4 acc[8][3];
  static_for<8>([&](auto t) { static_for<3>([&](auto u) { acc[t][u] = f32x4{0, 0, 0, 0}; }); });
  bf16x8 fa[8], fb[2][3];
  struct Sch { int cum[22]; int endU[21]; };
  constexpr Sch sch = [] { Sch r{}; int n = 0;
    n += 7; r.endU[0] = n; n += 4; r.endU[1] = n; n += 7; r.endU[2] = n; n += 4; r.endU[3] = n;
    for (int s = 0; s <= 20; ++s) { r.cum[s] = n;
      if (s + 4 < 20) { if ((s & 1) == 0) n += 3; n += 4; r.endU[s + 4] = n; }
      if (s == S_KA || s == S_VA) n += 6; }
    r.cum[21] = n; return r; }();
  wait_vm_bar<sch.cum[0] - sch.endU[0]>();
  STAMP(1); CSTAMP(28);
  static_for<3>([&](auto u) { fb[0][u] = *(LAS bf16x8*)(aX[0] + u * 2048); if (ABL_NOLDS) fb[1][u] = *(LAS bf16x8*)(aX[1] + u * 2048); });
  static_for<8>([&](auto t) { fa[t] = cat(trrd(aWq[t]), trrd(aWq[t] + 2048)); });
  static_for<20>([&](auto s_) { constexpr int s = s_, n = s + 1;
    if constexpr (s == 18) { if (par) wait_vm_bar<sch.cum[s] - sch.endU[n] + 12>(); else wait_vm_bar<sch.cum[s] - sch.endU[n]>(); }
    else if constexpr (n < 20 && !ABL_NOBAR) wait_vm_bar<sch.cum[s] - sch.endU[n]>();
    static_for<8>([&](auto t_) { constexpr int t = t_;
      acc[t][0] = mfma(fa[t], fb[s & 1][0], acc[t][0]); SB();
      if constexpr (n < 20 && t < 3 && !ABL_NOLDS) { fb[n & 1][t] = *(LAS bf16x8*)(aX[n & 1] + ((n >> 1) % 3) * 12288 + t * 2048); SB(); }
      if constexpr (s + 4 < 20 && !ABL_NODMA) {
        if constexpr ((s & 1) == 0) { if constexpr (t < 3) pieceE(IC<(s + 4) / 2>{}, IC<t>{}); else if constexpr (t < 7) pieceU(IC<s + 4>{}, IC<t - 3>{}); }
        else if constexpr (t < 4) pieceU(IC<s + 4>{}, IC<t>{});
        SB(); }
      acc[t][1] = mfma(fa[t], fb[s & 1][1], acc[t][1]); SB();
      acc[t][2] = mfma(fa[t], fb[s & 1][2], acc[t][2]); SB();
      if constexpr (n < 20 && !ABL_NOLDS) { const unsigned a = aWq[t] + (n & 3) * 16384; fa[t] = cat(trrd(a), trrd(a + 2048)); SB(); }
    });
    if constexpr (s == S_KA) static_for<6>([&](auto j) { dma<NT_W>(wk + (L_KA + 4096 * j), vK, rK, j * 4096); });
    if constexpr (s == S_VA) static_for<6>([&](auto j) { dma<NT_W>(wk + (L_VA + 4096 * j), vV, rV, j * 4096); });
    if constexpr (s == 17) { if (par) static_for<12>([&](auto j) { dma<NT_W>(qh * 1024 + (L_KB + 2048 * j), vKB[j & 1], rK, 24576 + (j >> 1) * 4096); }); }
    if constexpr (s == 18) { if (par) static_for<12>([&](auto j) { dma<NT_W>(qh * 1024 + (L_VB + 2048 * j), vVB, rV, 24576 + j * 2048); }); }
  });
  pin(acc);
  STAMP(2); CSTAMP(29);
  constexpr int NZ = 7;
  constexpr unsigned ZU[NZ][2] = {{49152, 59392}, {69632, 79872}, {90112, L_SP}, {L_KA, L_KA + 10240}, {L_KA + 20480, L_KA + 30720}, {0, 10240}, {20480, 30720}};
  unsigned vWo[5];
  static_for<5>([&](auto i) { const unsigned e = i * 64 + lane, row = e / 20, pos = e - row * 20; vWo[i] = (row + 16 * qh) * 1280 + ((pos ^ (((row >> 2) & 1u) << 1)) << 4); });
  auto p2_row = [&](int i, unsigned pr) -> unsigned {
    if (i < 4) return (2 * hp + pr) * 4 + i; const unsigned a = (i - 4) % 6 + 6 * pr; return (2 * ((hp + 1 + (a >> 2)) & 3u) + (i >= 10 ? 1u : 0u)) * 4 + (a & 3u); };
  auto pieceZ = [&](auto i_, unsigned pr, unsigned ld, auto j) { constexpr int i = i_; dma<NT_W>(ld + j * 1024, vWo[j], rWo, p2_row(i, pr) * (32 * 1280)); };
  wait_vm_bar<63>();
  if (par) static_for<3>([&](auto i) { static_for<2>([&](auto pr) { static_for<5>([&](auto j) { pieceZ(i, pr, ZU[i][pr] + qh * 5120, j); }); }); });

  bf16x8 qf[4][3];
  static_for<4>([&](auto s) { static_for<3>([&](auto u) { qf[s][u] = pack8(acc[2 * s][u], acc[2 * s + 1][u]); }); });
  SB(); pair_sync<42>(par, L_SYNC + w * 4, L_SYNC + (w ^ 1u) * 4, 1); SB();
  STAMP(3);
  f32x4 st[6][3];
  { const unsigned aK0 = (par ? L_KB : L_KA) + c * 256 + (((g >> 1) ^ c) << 4) + 8 * (g & 1u);
    bf16x8 kf[4];
    auto rdK = [&](auto i_) { constexpr int i = i_, kt = i >> 2, s = i & 3;
      kf[s] = cat(*(LAS bf16x4*)((aK0 ^ (64 * s)) + kt * 4096), *(LAS bf16x4*)((aK0 ^ (64 * s + 32)) + kt * 4096)); };
    static_for<4>([&](auto i) { rdK(i); });
    SB();
    static_for<24>([&](auto i_) { constexpr int i = i_, kt = i >> 2, s = i & 3;
      static_for<3>([&](auto u) { st[kt][u] = mfma(kf[s], qf[s][u], s == 0 ? f32x4{0, 0, 0, 0} : st[kt][u]); });
      SB();
      if constexpr (i + 4 < 24) { rdK(IC<i + 4>{}); SB(); } }); }
  bf16x8 pf[3][3];
  constexpr float CS = 0.08838834764831845f * 1.4426950408889634f;
  static_for<3>([&](auto u) {
    float m = st[0][u][0];
    static_for<6>([&](auto kt) { static_for<4>([&](auto r) { m = fmaxf(m, st[kt][u][(int)r]); }); });
    m = xmax32(xmax16(m));
    const float mc = -m * CS; float l = 0.f;
    static_for<6>([&](auto kt) { static_for<4>([&](auto r) { const float e = __builtin_amdgcn_exp2f(fmaf(st[kt][u][(int)r], CS, mc)); st[kt][u][(int)r] = e; l += e; }); });
    l = xsum32(xsum16(l));
    const float il = __builtin_amdgcn_rcpf(l);
    static_for<3>([&](auto s) { pf[s][u] = pack8(st[2 * s][u] * il, st[2 * s + 1][u] * il); });
  });
  STAMP(4);
  SB(); pair_sync<30>(par, L_SYNC + w * 4, L_SYNC + (w ^ 1u) * 4, 2); SB();
  STAMP(5);
  const unsigned fA = WS_FO + (((cluster * 4 + hg) * 4 + ((hp + 1 + ((6 * par + (lane & 1u) * 5) >> 2)) & 3u)) * 4 + qh) * 128, fB = fA + 256;
  bf16x8 of[4][3];
  { f32x4 ot[8][3];
    const unsigned aV0 = (par ? L_VB : L_VA) + (4 * g + q4) * 256 + F * 32 + 8 * pp;
    bf16x8 vf[4];
    auto rdV = [&](auto i_) { constexpr int i = i_, t = i / 3, s = i % 3; const unsigned a = (aV0 ^ (t << 5)) + s * 8192; vf[i & 3] = cat(trrd(a), trrd(a + 4096)); };
    static_for<4>([&](auto i) { rdV(i); });
    SB();
    const unsigned ob = WS_O + ((cluster * 32 + hA + par) * 2 + qh) * 12288;
    static_for<24>([&](auto i_) { constexpr int i = i_, t = i / 3, s = i % 3;
      static_for<3>([&](auto u) { ot[t][u] = mfma(vf[i & 3], pf[s][u], s == 0 ? f32x4{0, 0, 0, 0} : ot[t][u]); });
      SB();
      if constexpr (i + 4 < 24) { rdV(IC<i + 4>{}); SB(); }
      if constexpr (i >= 8 && (i - 8) % 6 < 3) { constexpr int k = (i - 8) / 6, u = (i - 8) % 6;
        of[k][u] = pack8(ot[2 * k][u], ot[2 * k + 1][u]); st_sc1<u * 1024>(of[k][u], lane * 16, rWs, ob + k * 3072); SB(); } });
    static_for<3>([&](auto u) { of[3][u] = pack8(ot[6][u], ot[7][u]); st_sc1<u * 1024>(of[3][u], lane * 16, rWs, ob + 3 * 3072); });
  }
  STAMP(6);
  u64 fv;
  flag_ld(fv, fA, rWs);
  wait_vm<1>();
  if (lane == 0) flag_st(tag, WS_FO + (((cluster * 4 + hg) * 4 + hp) * 4 + w) * 128, rWs);
  STAMP(7);
  asm volatile("s_waitcnt vmcnt(1)" : "+v"(fv) :: "memory");
  flag_spin(fv, fA, rWs, tag);
  STAMP(8);
  bf16x8 ob[5][3];
  auto ldO = [&](auto i_) { constexpr int i = i_; const unsigned rw = p2_row(i, par), so = WS_O + ((cluster * 32 + 8 * hg + (rw >> 2)) * 2 + qh) * 12288 + (rw & 3u) * 3072;
    static_for<3>([&](auto u) { ld_sc1<u * 1024>(ob[(i - 4) % 5][u], lane * 16, rWs, so); }); };
  ldO(IC<4>{});
  wait_vm_bar<3>();
  STAMP(9); CSTAMP(30);
  const unsigned zq = qh * 5120;
  static_for<3>([&](auto k) { static_for<5>([&](auto j) { pieceZ(IC<3 + k>{}, par, (par ? ZU[3 + k][1] : ZU[3 + k][0]) + zq, j); }); });
  f32x4 a2[10][3];
  const unsigned aZe = (4 * g + q4) * 320 + (g & 1u) * 32 + 8 * pp, aZo = (4 * g + q4) * 320 - (g & 1u) * 32 + 8 * pp;
  bf16x8 af[5];
  auto rdZ = [&](auto f_) { constexpr int f = f_, i = f / 10, t = f % 10; const unsigned a = ((t & 1) ? aZo : aZe) + (par ? ZU[i % NZ][1] : ZU[i % NZ][0]) + t * 32; af[f % 5] = cat(trrd(a), trrd(a + 5120)); };
  static_for<5>([&](auto t) { rdZ(t); });
  struct Sch2 { int cum[17], endZ[16], endO[16], endF; };
  constexpr Sch2 s2 = [] { Sch2 r{}; int n = 0;
    for (int i = 4; i < 5; ++i) { n += 3; r.endO[i] = n; }
    for (int i = 0; i < 3; ++i) r.endZ[i] = 0;
    for (int i = 3; i < 6; ++i) { n += 5; r.endZ[i] = n; }
    for (int i = 0; i < 16; ++i) { r.cum[i] = n;
      if (i >= 1 && i <= 5) { n += 3; r.endO[i + 4] = n; }
      if (i == 8) for (int k = 10; k < 13; ++k) { n += 3; r.endO[k] = n; }
      if (i >= 9 && i <= 11) { n += 3; r.endO[i + 4] = n; }
      if (i + 6 < 16) { n += 5; r.endZ[i + 6] = n; }
      if (i == 7) { n += 1; r.endF = n; } }
    r.cum[16] = n; return r; }();
  static_for<16>([&](auto i_) { constexpr int i = i_, n = i + 1;
    constexpr int need0 = n < 16 ? s2.endZ[n] : 0, need1 = i >= 4 ? s2.endO[i] : 0, need2 = i == 8 ? s2.endF : 0;
    constexpr int need = need0 > need1 ? (need0 > need2 ? need0 : need2) : (need1 > need2 ? need1 : need2);
    wait_vm_bar<s2.cum[i] - need>();
    if constexpr (i >= 4) asm volatile("" : "+v"(ob[(i - 4) % 5][0]), "+v"(ob[(i - 4) % 5][1]), "+v"(ob[(i - 4) % 5][2]));
    if constexpr (i == 8) { asm volatile("" : "+v"(fv)); flag_spin(fv, fB, rWs, tag); STAMP(10); ldO(IC<10>{}); ldO(IC<11>{}); ldO(IC<12>{}); SB(); }
    if constexpr (i >= 1 && i <= 5) { ldO(IC<i + 4>{}); SB(); }
    if constexpr (i >= 9 && i <= 11) { ldO(IC<i + 4>{}); SB(); }
    static_for<10>([&](auto t_) { constexpr int t = t_, f = 10 * i + t;
      static_for<3>([&](auto u) { a2[t][u] = mfma(af[f % 5], i < 4 ? of[i & 3][u] : ob[(i + 1) % 5][u], i == 0 ? f32x4{0, 0, 0, 0} : a2[t][u]); SB();
        if constexpr (u == 0 && i + 6 < 16 && t < 5) { pieceZ(IC<i + 6>{}, par, (par ? ZU[(i + 6) % NZ][1] : ZU[(i + 6) % NZ][0]) + zq, IC<t>{}); SB(); } });
      if constexpr (f + 5 < 160) { rdZ(IC<f + 5>{}); SB(); } });
    if constexpr (i == 7) flag_ld(fv, fB, rWs);
  });
  pin(a2);
  STAMP(11); CSTAMP(31);
  f32x4 r[5][3];
  wait_vm_bar<63>();
  if (par) static_for<15>([&](auto i) { *(LAS f32x4*)(w * 15360 + i * 1024 + lane * 16) = a2[i / 3][i % 3]; r[i / 3][i % 3] = a2[5 + i / 3][i % 3]; });
  else     static_for<15>([&](auto i) { *(LAS f32x4*)(w * 15360 + i * 1024 + lane * 16) = a2[5 + i / 3][i % 3]; r[i / 3][i % 3] = a2[i / 3][i % 3]; });
  wait_vm_bar<63>();
  static_for<15>([&](auto i) { r[i / 3][i % 3] += *(LAS f32x4*)((w ^ 2u) * 15360 + i * 1024 + lane * 16); });
  STAMP(12);
  const unsigned pbase = WS_P + (cluster * 4 + hp) * 16 * 15360;
  if (w != hg) {
    static_for<15>([&](auto i) { st_sc1<(i & 3) * 1024>(r[i / 3][i % 3], lane * 16, rWs, pbase + (w * 4 + hg) * 15360 + (i >> 2) * 4096); });
    wait_vm<0>();
    if (lane == 0) flag_st(tag, WS_FP + (((cluster * 4 + hp) * 4 + w) * 4 + hg) * 128, rWs);
    STAMP(13);
  } else {
    const unsigned fP = WS_FP + (((cluster * 4 + hp) * 4 + hg) * 4 + ((hg + 1 + lane % 3u) & 3u)) * 128;
    flag_spin(tag + 1, fP, rWs, tag);
    STAMP(13);
    bf16x8 in[2][15];
    auto ldP = [&](auto o) { const unsigned src = (hg + 1 + o) & 3u;
      static_for<15>([&](auto i) { ld_sc1<(i & 3) * 1024>(in[o & 1][i], lane * 16, rWs, pbase + (hg * 4 + src) * 15360 + (i >> 2) * 4096); }); };
    auto addP = [&](auto o) { static_for<15>([&](auto i) { asm volatile("" : "+v"(in[o & 1][i])); r[i / 3][i % 3] += __builtin_bit_cast(f32x4, in[o & 1][i]); }); SB(); };
    ldP(IC<0>{}); ldP(IC<1>{}); wait_vm<15>(); addP(IC<0>{}); ldP(IC<2>{}); wait_vm<15>(); addP(IC<1>{}); wait_vm<0>(); addP(IC<2>{});
    STAMP(14);
    char* o = p.out + ((size_t)(b * 96 + 48 * qh + c) * 1280 + br * 640 + hp * 160 + par * 80 + 4 * g) * 2;
    static_for<5>([&](auto t) { static_for<3>([&](auto u) { u32x2 v = {cvtpk(r[t][u][0], r[t][u][1]), cvtpk(r[t][u][2], r[t][u][3])}; *(u32x2*)(o + u * 16 * 2560 + t * 32) = v; }); });
    if (hg == 0 && hp == 0 && lane == 0) flag_st(tag, WS_EPOCH + cluster * 256, rWs);
    STAMP(15);
  }
  if (w == hg && lane == 0) { *(volatile unsigned*)(ws + WS_STAMP + blockIdx.x * 8) = dI_; *(volatile unsigned*)(ws + WS_STAMP + blockIdx.x * 8 + 4) = (unsigned)__builtin_amdgcn_s_memrealtime(); }
}

extern "C" void kernel_launch(void* const* d_in, const int* in_sizes, int n_in, void* d_out, int out_size, void* d_ws, size_t ws_size, hipStream_t stream) {
  Params p;
  for (int i = 0; i < 2; ++i) { p.x[i] = (const char*)d_in[5 * i]; p.k[i] = (const char*)d_in[5 * i + 1]; p.v[i] = (const char*)d_in[5 * i + 2]; p.wq[i] = (const char*)d_in[5 * i + 3]; p.wo[i] = (const char*)d_in[5 * i + 4]; }
  p.out = (char*)d_out; p.ws = (char*)d_ws;
  (void)hipFuncSetAttribute((const void*)fused, hipFuncAttributeMaxDynamicSharedMemorySize, LDS_BYTES);
  fused<<<256, 256, LDS_BYTES, stream>>>(p);
  postk<<<1, 256, 0, stream>>>(p.ws, 0);
}
```

```cpp
#include <hip/hip_runtime.h>
#include <hip/hip_bf16.h>
#include <utility>

typedef __bf16   bf16x8 __attribute__((ext_vector_type(8)));
typedef __bf16   bf16x4 __attribute__((ext_vector_type(4)));
typedef __bf16   bf16x2 __attribute__((ext_vector_type(2)));
typedef float    f32x4  __attribute__((ext_vector_type(4)));
typedef float    f32x2  __attribute__((ext_vector_type(2)));
typedef unsigned u32x16 __attribute__((ext_vector_type(16)));
typedef unsigned u32x4  __attribute__((ext_vector_type(4)));
typedef unsigned u32x2  __attribute__((ext_vector_type(2)));
typedef unsigned long long u64;
typedef __amdgpu_buffer_rsrc_t rsrc_t;
#define LAS __attribute__((address_space(3)))
#define DI __device__ __forceinline__
#define SB() __builtin_amdgcn_sched_barrier(0)
template <int N> using IC = std::integral_constant<int, N>;
template <int... Is, class F> DI void static_for_impl(std::integer_sequence<int, Is...>, F&& f) { (f(IC<Is>{}), ...); }
template <int N, class F> DI void static_for(F&& f) { static_for_impl(std::make_integer_sequence<int, N>{}, f); }

constexpr int SQ = 96, DM = 640, NH = 32, DH = 128, HD = 4096;
constexpr unsigned WS_EPOCH = 0;
constexpr unsigned WS_FO    = 4096;
constexpr unsigned WS_FO2   = 16 * 8 * 128;
constexpr unsigned WS_FP    = WS_FO + 2 * WS_FO2;
constexpr unsigned WS_O     = 1u << 20;
constexpr unsigned WS_P     = WS_O + 16 * 32 * 2 * 12288;
constexpr unsigned WS_END   = WS_P + 16 * 4 * 4 * 4 * 8192;
constexpr unsigned L_WQ = 0;
constexpr unsigned L_X  = 65536;
constexpr unsigned L_CNT = 110592;
constexpr unsigned L_K = 114688, L_V = 0, L_OO = 114688, L_OL = 20480, L_OWN = 20480;
constexpr unsigned LDS_BYTES = 163840;
constexpr unsigned ZU[8] = {49152, 59392, 69632, 79872, 90112, 100352, 0, 10240};
constexpr unsigned oslot(int k) { return (k & 4) ? L_OO + (k & 3) * 6144 : L_OL + (k & 3) * 6144; }

DI unsigned cvtpk(float lo, float hi) { f32x2 v = {lo, hi}; bf16x2 b = __builtin_convertvector(v, bf16x2); return __builtin_bit_cast(unsigned, b); }
DI bf16x8 pack8(const f32x4& a, const f32x4& b) { u32x4 w = {cvtpk(a[0], a[1]), cvtpk(a[2], a[3]), cvtpk(b[0], b[1]), cvtpk(b[2], b[3])}; return __builtin_bit_cast(bf16x8, w); }
DI f32x4 mfma(bf16x8 a, bf16x8 b, f32x4 c) { return __builtin_amdgcn_mfma_f32_16x16x32_bf16(a, b, c, 0, 0, 0); }
DI bf16x4 trrd(unsigned addr) { return __builtin_amdgcn_ds_read_tr16_b64_v4bf16((LAS bf16x4*)addr); }
DI bf16x8 cat(bf16x4 a, bf16x4 b) { return __builtin_shufflevector(a, b, 0, 1, 2, 3, 4, 5, 6, 7); }
DI f32x2 lo2(const f32x4& v) { return __builtin_shufflevector(v, v, 0, 1); }
DI f32x2 hi2(const f32x4& v) { return __builtin_shufflevector(v, v, 2, 3); }
DI float max2(float a, float b) { float r; asm("v_max_f32 %0, %1, %2" : "=v"(r) : "v"(a), "v"(b)); return r; }
DI float max3(float a, float b, float c) { float r; asm("v_max3_f32 %0, %1, %2, %3" : "=v"(r) : "v"(a), "v"(b), "v"(c)); return r; }
DI float xmax16(float v) { auto r = __builtin_amdgcn_permlane16_swap(__float_as_uint(v), __float_as_uint(v), false, false); return max2(__uint_as_float(r[0]), __uint_as_float(r[1])); }
DI float xmax32(float v) { auto r = __builtin_amdgcn_permlane32_swap(__float_as_uint(v), __float_as_uint(v), false, false); return max2(__uint_as_float(r[0]), __uint_as_float(r[1])); }
DI float xsum16(float v) { auto r = __builtin_amdgcn_permlane16_swap(__float_as_uint(v), __float_as_uint(v), false, false); return __uint_as_float(r[0]) + __uint_as_float(r[1]); }
DI float xsum32(float v) { auto r = __builtin_amdgcn_permlane32_swap(__float_as_uint(v), __float_as_uint(v), false, false); return __uint_as_float(r[0]) + __uint_as_float(r[1]); }
DI rsrc_t mkrsrc(const void* p, unsigned bytes) { return __builtin_amdgcn_make_buffer_rsrc((void*)p, 0, bytes, 0x00020000); }
template <class T> DI void pin3(T (&a)[3]) { asm volatile("" : "+v"(a[0]), "+v"(a[1]), "+v"(a[2])); }
template <int N, class T> DI void pin(T (&a)[N][3]) { static_for<N>([&](auto i) { pin3(a[i]); }); }

DI void set_m0(unsigned base, unsigned add) { asm volatile("s_add_u32 m0, %0, %1" :: "s"(base), "i"(add) : "scc", "memory"); }
DI void set_m0r(unsigned v) { asm volatile("s_mov_b32 m0, %0\n\ts_nop 0" :: "s"(v) : "memory"); }
DI void dma_nt(unsigned lds, unsigned voff, rsrc_t r, unsigned soff) {
  asm volatile("s_mov_b32 m0, %0\n\ts_nop 0\n\tbuffer_load_dwordx4 %1, %2, %3 offen nt lds" :: "s"(lds), "v"(voff), "s"(r), "s"(soff) : "memory"); }
template <int OFF> DI void dma(unsigned voff, rsrc_t r, unsigned soff) {
  asm volatile("buffer_load_dwordx4 %0, %1, %2 offen offset:%3 lds" :: "v"(voff), "s"(r), "s"(soff), "i"(OFF) : "memory"); }
template <int OFF> DI void dma_sc1(unsigned voff, rsrc_t r, unsigned soff) {
  asm volatile("buffer_load_dwordx4 %0, %1, %2 offen offset:%3 sc1 lds" :: "v"(voff), "s"(r), "s"(soff), "i"(OFF) : "memory"); }
DI void touch(unsigned& d, unsigned voff, rsrc_t r) { asm volatile("buffer_load_dword %0, %1, %2, 0 offen" : "+v"(d) : "v"(voff), "s"(r) : "memory"); }
template <int OFF> DI void ld_plain(bf16x8& d, unsigned voff, rsrc_t r, unsigned soff) { asm volatile("buffer_load_dwordx4 %0, %1, %2, %3 offen offset:%4" : "=v"(d) : "v"(voff), "s"(r), "s"(soff), "i"(OFF) : "memory"); }
template <int OFF> DI void ld_sc1(bf16x8& d, unsigned voff, rsrc_t r, unsigned soff) { asm volatile("buffer_load_dwordx4 %0, %1, %2, %3 offen offset:%4 sc1" : "=v"(d) : "v"(voff), "s"(r), "s"(soff), "i"(OFF) : "memory"); }
template <int OFF, class T> DI void st_sc1(const T& d, unsigned voff, rsrc_t r, unsigned soff) { asm volatile("buffer_store_dwordx4 %0, %1, %2, %3 offen offset:%4 sc1\n\ts_nop 1" :: "v"(d), "v"(voff), "s"(r), "s"(soff), "i"(OFF) : "memory"); }
DI void st_out(const u32x4& d, unsigned voff, rsrc_t r, unsigned soff) { asm volatile("buffer_store_dwordx4 %0, %1, %2, %3 offen\n\ts_nop 1" :: "v"(d), "v"(voff), "s"(r), "s"(soff) : "memory"); }
template <int N> DI void wait_vm() { asm volatile("s_waitcnt vmcnt(%0)" :: "i"(N) : "memory"); }
template <int N> DI void vm_bar() { SB(); asm volatile("s_waitcnt vmcnt(%0)\n\ts_barrier" :: "i"(N) : "memory"); SB(); }
DI void bar() { SB(); asm volatile("s_barrier" ::: "memory"); SB(); }
DI void lds_bar() { SB(); __builtin_amdgcn_s_waitcnt(0xC07F); asm volatile("s_barrier" ::: "memory"); SB(); }
DI unsigned rdlane(unsigned v, int l) { unsigned r = __builtin_amdgcn_readlane(v, l); asm volatile("s_nop 4" : "+s"(r)); return r; }
DI u64 sload64(const char* ptr) { u64 v; asm volatile("s_load_dwordx2 %0, %1, 0x0\n\ts_waitcnt lgkmcnt(0)" : "=s"(v) : "s"(ptr) : "memory"); return v; }
DI void cnt_add(unsigned addr) { u64 sv; unsigned one = 1; asm volatile("s_mov_b64 %0, exec\n\ts_mov_b64 exec, 1\n\tds_add_u32 %1, %2\n\ts_mov_b64 exec, %0" : "=&s"(sv) : "v"(addr), "v"(one) : "memory"); }
DI void cnt_wait(unsigned addr, unsigned val) { unsigned cnt = 1u << 22, t;
  asm volatile("1:\n\tds_read_b32 %[t], %[a]\n\ts_waitcnt lgkmcnt(0)\n\tv_cmp_le_u32 vcc, %[val], %[t]\n\ts_nop 1\n\ts_cmp_eq_u64 vcc, exec\n\ts_cbranch_scc1 2f\n\ts_sub_u32 %[cnt], %[cnt], 1\n\ts_cbranch_scc0 1b\n2:"
               : [t] "=&v"(t), [cnt] "+s"(cnt) : [a] "v"(addr), [val] "s"(val) : "vcc", "scc", "memory"); }
template <int N> DI void post(unsigned f, rsrc_t r, u64 tag) { const u32x2 tg = {(unsigned)tag, (unsigned)(tag >> 32)}; u64 sv;
  asm volatile("s_waitcnt vmcnt(%[n])\n\ts_mov_b64 %[sv], exec\n\ts_mov_b64 exec, 1\n\tbuffer_store_dwordx2 %[tg], off, %[r], %[f] sc1\n\ts_mov_b64 exec, %[sv]"
               : [sv] "=&s"(sv) : [tg] "v"(tg), [r] "s"(r), [f] "s"(f), [n] "i"(N) : "memory"); }
DI void flag_spin(unsigned voff, rsrc_t r, u64 tag) { unsigned cnt = 1u << 20; u64 v;
  asm volatile("1:\n\tbuffer_load_dwordx2 %[v], %[off], %[r], 0 offen sc1\n\ts_waitcnt vmcnt(0)\n\tv_cmp_eq_u64 vcc, %[tag], %[v]\n\ts_nop 1\n\ts_cmp_eq_u64 vcc, exec\n\ts_cbranch_scc1 2f\n\t"
               "s_sub_u32 %[cnt], %[cnt], 1\n\ts_cbranch_scc1 2f\n\ts_sleep 1\n\ts_branch 1b\n2:"
               : [v] "=&v"(v), [cnt] "+s"(cnt) : [tag] "s"(tag), [off] "v"(voff), [r] "s"(r) : "vcc", "scc", "memory"); }
#define PEEK_CMP "s_waitcnt lgkmcnt(0)\n\ts_xor_b64 s[80:81], s[80:81], %[tag]\n\ts_xor_b64 s[82:83], s[82:83], %[tag]\n\ts_xor_b64 s[84:85], s[84:85], %[tag]\n\ts_xor_b64 s[86:87], s[86:87], %[tag]\n\t" \
                 "s_xor_b64 s[88:89], s[88:89], %[tag]\n\ts_xor_b64 s[90:91], s[90:91], %[tag]\n\ts_xor_b64 s[92:93], s[92:93], %[tag]\n\ts_xor_b64 s[94:95], s[94:95], %[tag]\n\t" \
                 "s_or_b64 s[80:81], s[80:81], s[82:83]\n\ts_or_b64 s[84:85], s[84:85], s[86:87]\n\ts_or_b64 s[88:89], s[88:89], s[90:91]\n\ts_or_b64 s[92:93], s[92:93], s[94:95]\n\t" \
                 "s_or_b64 s[80:81], s[80:81], s[84:85]\n\ts_or_b64 s[88:89], s[88:89], s[92:93]\n\ts_or_b64 s[80:81], s[80:81], s[88:89]\n\t"
DI void peek_issue(u32x16& f, const char* line) { asm volatile("s_load_dwordx16 s[80:95], %1, 0x0 glc" : "+{s[80:95]}"(f) : "s"(line) : "memory"); }
DI unsigned peek_check(u32x16& f, u64 tag) { unsigned r;
  asm volatile(PEEK_CMP "s_cselect_b32 %[r], 1, 0" : "+{s[80:95]}"(f), [r] "=s"(r) : [tag] "s"(tag) : "scc", "memory"); return r; }
template <bool AGAIN> DI void peek_step(u32x16& f, unsigned& got, u64 tag, const char* line, unsigned voff, rsrc_t r, unsigned s0, unsigned s1, unsigned m0a, unsigned m0b) {
  asm volatile("s_cmp_lg_u32 %[got], 0\n\ts_cbranch_scc1 9f\n\t" PEEK_CMP "s_cbranch_scc1 8f\n\t"
               "s_mov_b32 m0, %[ma]\n\ts_mov_b32 %[got], 1\n\tbuffer_load_dwordx4 %[v], %[r], %[s0] offen sc1 lds\n\tbuffer_load_dwordx4 %[v], %[r], %[s0] offen offset:1024 sc1 lds\n\tbuffer_load_dwordx4 %[v], %[r], %[s0] offen offset:2048 sc1 lds\n\t"
               "s_mov_b32 m0, %[mb]\n\ts_nop 0\n\tbuffer_load_dwordx4 %[v], %[r], %[s1] offen sc1 lds\n\tbuffer_load_dwordx4 %[v], %[r], %[s1] offen offset:1024 sc1 lds\n\tbuffer_load_dwordx4 %[v], %[r], %[s1] offen offset:2048 sc1 lds\n\t"
               "s_branch 9f\n8:\n\t.if %[ag]\n\ts_load_dwordx16 s[80:95], %[line], 0x0 glc\n\t.endif\n9:"
               : "+{s[80:95]}"(f), [got] "+s"(got) : [tag] "s"(tag), [line] "s"(line), [v] "v"(voff), [r] "s"(r), [s0] "s"(s0), [s1] "s"(s1), [ma] "s"(m0a), [mb] "s"(m0b), [ag] "i"(AGAIN ? 1 : 0) : "scc", "memory"); }

struct Params { const char *x[2], *k[2], *v[2], *wq[2], *wo[2]; char* out; char* ws; };

__global__ __launch_bounds__(512, 2) void fused(Params p) {
  const unsigned tid = threadIdx.x, lane = tid & 63u, w8 = __builtin_amdgcn_readfirstlane(tid >> 6), w = w8 & 3u;
  const unsigned id = blockIdx.x, xcd = id & 7u, slot = id >> 3, br = xcd >> 2, hg = xcd & 3u, b = slot >> 2, hp = slot & 3u;
  const unsigned par = w >> 1, qh = w & 1u, hA = 8 * hg + 2 * hp, cluster = br * 8 + b;
  const unsigned g = lane >> 4, c = lane & 15u, q4 = c >> 2, pp = lane & 3u, l16 = lane * 16;
  asm volatile("fused_text_begin:");
  if (__builtin_expect(w8 < 4, 0)) {
    char* ws = p.ws; char* pout = p.out;
    asm volatile("" : "+s"(ws), "+s"(pout));
    const u64 tag = sload64(ws + WS_EPOCH + cluster * 256) + 1;
    const rsrc_t rWs = mkrsrc(ws, WS_END);
    if (tid < 2) *(LAS unsigned*)(L_CNT + tid * 4) = 0;
    const unsigned F = q4 | ((g & 1u) << 2);
    unsigned aWq[8], aK[8], aV[8], aZ[2][2], aO[8], aF[2];
    static_for<8>([&](auto t) { aWq[t] = (L_WQ + (8 * g + q4) * 512 + par * 256 + F * 32 + 8 * pp) ^ (t << 5); });
    const unsigned aX0 = L_X + (48 * qh + c) * 128 + ((g ^ ((c >> 1) & 7u)) << 4), aX1 = aX0 ^ 64u;
    static_for<2>([&](auto k) { static_for<2>([&](auto e) { aZ[k][e] = k * 57344 + (4 * g + q4) * 320 + (((5 * par + e) ^ (g & 1u)) - e) * 32 + 8 * pp; asm volatile("" : "+v"(aZ[k][e])); }); });
    static_for<8>([&](auto j) { const unsigned ix = (j + b) & 7u; aO[j] = L_OO + (ix >> 2) * 24576 + qh * 12288 + (ix & 3u) * 3072 + l16; asm volatile("" : "+v"(aO[j])); });
    aF[0] = L_OL + qh * 3072 + l16; aF[1] = L_OO + qh * 3072 + l16; asm volatile("" : "+v"(aF[0]), "+v"(aF[1]));
    static_for<8>([&](auto i) { aK[i] = (L_K + par * 24576 + c * 256 + (((g >> 1) ^ c) << 4) + 8 * (g & 1u)) ^ (32 * i);
                                aV[i] = (L_V + par * 24576 + (4 * g + q4) * 256 + F * 32 + 8 * pp) ^ (32 * i); asm volatile("" : "+v"(aK[i]), "+v"(aV[i])); });
    f32x4 acc[8][3];
    static_for<8>([&](auto t) { static_for<3>([&](auto u) { acc[t][u] = f32x4{0, 0, 0, 0}; }); });
    pin(acc);
    bf16x4 al[2][8], ah[2][8]; bf16x8 fb[2][3];
    lds_bar();
    static_for<3>([&](auto u) { fb[0][u] = *(LAS bf16x8*)(aX0 + u * 2048); });
    static_for<8>([&](auto t) { al[0][t] = trrd(aWq[t]); ah[0][t] = trrd(aWq[t] + 2048); });
    unsigned xo = 0, aXn = aX1;
    bf16x8 qf[2][3]; u32x4 qn[3];
    bf16x4 kl[4], kh[4];
    auto rdKL = [&](auto i_) { constexpr int i = i_; kl[i & 3] = *(LAS bf16x4*)(aK[2 * (i / 6)] + (i % 6) * 4096); };
    auto rdKH = [&](auto i_) { constexpr int i = i_; kh[i & 3] = *(LAS bf16x4*)(aK[2 * (i / 6) + 1] + (i % 6) * 4096); };
    auto packQ = [&](auto s_, auto e_) { constexpr int s = s_, e = e_, uu = e >> 2, d = e & 3; const f32x4& src = acc[2 * s + (d >> 1)][uu]; qn[uu][d] = cvtpk(src[2 * (d & 1)], src[2 * (d & 1) + 1]);
      if constexpr (d == 3) qf[s & 1][uu] = __builtin_bit_cast(bf16x8, qn[uu]); };
    auto qstep = [&](auto s_, auto last) { constexpr int s = s_, n = s + 1;
      static_for<24>([&](auto m_) { constexpr int m = m_, t = m / 3, u = m % 3;
        acc[t][u] = mfma(cat(al[s & 1][t], ah[s & 1][t]), fb[s & 1][u], acc[t][u]); SB();
        if constexpr (last) { if constexpr (m < 8) { if constexpr (m & 1) rdKH(IC<m / 2>{}); else rdKL(IC<m / 2>{}); } else if constexpr (m >= 10 && m < 22) packQ(IC<0>{}, IC<m - 10>{}); }
        else if constexpr (m < 3) fb[n & 1][m] = *(LAS bf16x8*)(aXn + m * 2048);
        else if constexpr (m < 19) { constexpr int r = m - 3; if constexpr (r & 1) ah[n & 1][r >> 1] = trrd(aWq[r >> 1] + (n & 3) * 16384 + 2048); else al[n & 1][r >> 1] = trrd(aWq[r >> 1] + (n & 3) * 16384); }
        else if constexpr (m == 20) {
          if constexpr (s & 1) aXn = aX1 + xo; else { xo += 12288; xo = xo == 36864u ? 0u : xo; aXn = aX0 + xo; }
          asm volatile("" : "+v"(aXn)); }
        SB(); }); };
#pragma unroll 1
    for (unsigned it = 0; it < 4; ++it) static_for<4>([&](auto s_) { lds_bar(); qstep(s_, IC<0>{}); });
    static_for<4>([&](auto s_) { lds_bar(); qstep(s_, IC<s_ == 3>{}); });
    f32x4 st[6][3];
    float mx[3];
    static_for<72>([&](auto m_) { constexpr int m = m_, i = m / 3, u = m % 3, s = i / 6, kt = i % 6, r = m % 18;
      st[kt][u] = mfma(cat(kl[i & 3], kh[i & 3]), qf[s & 1][u], s == 0 ? f32x4{0, 0, 0, 0} : st[kt][u]); SB();
      if constexpr (u == 0 && i > 0 && i + 3 < 24) rdKH(IC<i + 3>{});
      if constexpr (u == 2 && i + 4 < 24) rdKL(IC<i + 4>{});
      if constexpr (s < 3) { constexpr int e = u == 1 ? 2 * kt : u == 2 ? 2 * kt + 1 : -1; if constexpr (e >= 0) packQ(IC<s + 1>{}, IC<e>{}); }
      else if constexpr (r >= 4) {
        constexpr int kp_ = (r - 4) / 3, up = (r - 4) % 3; const f32x4& v = st[kp_][up];
        if constexpr (kp_ == 0) mx[up] = max3(max3(v[0], v[1], v[2]), v[3], v[3]); else mx[up] = max3(max3(mx[up], v[0], v[1]), v[2], v[3]); }
      SB(); });
    cnt_add(L_CNT + par * 4);
    bf16x8 pf[3][3];
    constexpr float CS = 0.08838834764831845f * 1.4426950408889634f;
    asm volatile("s_nop 3");
    { const f32x4& v = st[4][2]; mx[2] = max3(max3(mx[2], v[0], v[1]), v[2], v[3]); }
    static_for<3>([&](auto u) { const f32x4& v = st[5][u]; mx[u] = max3(max3(mx[u], v[0], v[1]), v[2], v[3]); SB(); });
    static_for<3>([&](auto u) { mx[u] = xmax16(mx[u]); });
    static_for<3>([&](auto u) { mx[u] = xmax32(mx[u]) * -CS; });
    float ls[3];
    {
      const f32x2 c2 = {CS, CS}; f32x2 sa[3], sb[3], ta[2], tb[2];
      auto F_ = [&](auto q) { const f32x2 m2 = {mx[q % 3], mx[q % 3]}; ta[q & 1] = __builtin_elementwise_fma(lo2(st[q / 3][q % 3]), c2, m2); tb[q & 1] = __builtin_elementwise_fma(hi2(st[q / 3][q % 3]), c2, m2); };
      auto A_ = [&](auto q) { if constexpr (q < 3) { sa[q] = lo2(st[0][q]); sb[q] = hi2(st[0][q]); } else { sa[q % 3] += lo2(st[q / 3][q % 3]); sb[q % 3] += hi2(st[q / 3][q % 3]); } };
      F_(IC<0>{}); SB();
      static_for<18>([&](auto q) {
        if constexpr (q < 17) F_(IC<q + 1>{});
        SB();
        st[q / 3][q % 3] = f32x4{__builtin_amdgcn_exp2f(ta[q & 1][0]), __builtin_amdgcn_exp2f(ta[q & 1][1]), __builtin_amdgcn_exp2f(tb[q & 1][0]), __builtin_amdgcn_exp2f(tb[q & 1][1])};
        SB();
        if constexpr (q > 0) A_(IC<q - 1>{});
        SB(); });
      A_(IC<17>{});
      static_for<3>([&](auto u) { sa[u] += sb[u]; }); static_for<3>([&](auto u) { ls[u] = sa[u][0] + sa[u][1]; }); }
    static_for<3>([&](auto u) { ls[u] = xsum16(ls[u]); });
    static_for<3>([&](auto u) { ls[u] = __builtin_amdgcn_rcpf(xsum32(ls[u])); });
    SB();
    static_for<3>([&](auto u) { st[0][u] *= ls[u]; st[1][u] *= ls[u]; }); SB();
    static_for<3>([&](auto s) {
      if constexpr (s < 2) static_for<3>([&](auto u) { st[2 * s + 2][u] *= ls[u]; st[2 * s + 3][u] *= ls[u]; });
      SB();
      static_for<3>([&](auto u) { pf[s][u] = pack8(st[2 * s][u], st[2 * s + 1][u]); }); SB(); });
    pin(pf);
    SB(); cnt_wait(L_CNT + par * 4, 6); SB();
    const unsigned og = WS_O + ((cluster * 32 + hA + par) * 2 + qh) * 12288;
    const unsigned fme = WS_FO + ((cluster * 4 + hg) * 2 + qh) * 128 + (hp * 2 + par) * 8;
    { f32x4 ot[8][3]; u32x4 on;
      const unsigned aOw = L_OO + w * 12288 + l16;
      bf16x4 vl[4], vh[4];
      auto rdL = [&](auto i_) { constexpr int i = i_; vl[i & 3] = trrd(aV[i / 3] + (i % 3) * 8192); };
      auto rdH = [&](auto i_) { constexpr int i = i_; vh[i & 3] = trrd(aV[i / 3] + (i % 3) * 8192 + 4096); };
      auto emit = [&](auto e_) { constexpr int e = e_, k = e / 18, u = (e % 18) / 6, d = e % 6;
        if constexpr (d < 4) { const f32x4& src = ot[2 * k + (d >> 1)][u]; on[d] = cvtpk(src[2 * (d & 1)], src[2 * (d & 1) + 1]); }
        else if constexpr (d == 4) *(LAS u32x4*)(aOw + (k * 3 + u) * 1024) = on;
        else st_sc1<u * 1024>(on, l16, rWs, og + k * 3072); };
      static_for<4>([&](auto i) { rdL(i); rdH(i); });
      SB();
      static_for<72>([&](auto m_) { constexpr int m = m_, i = m / 3, u = m % 3, t = i / 3, s = i % 3;
        ot[t][u] = mfma(cat(vl[i & 3], vh[i & 3]), pf[s][u], s == 0 ? f32x4{0, 0, 0, 0} : ot[t][u]); SB();
        if constexpr (u == 0 && i > 0 && i + 3 < 24) rdH(IC<i + 3>{});
        if constexpr (u == 2 && i + 4 < 24) rdL(IC<i + 4>{});
        if constexpr (m >= 19) emit(IC<m - 19>{});
        SB(); });
      static_for<19>([&](auto e) { emit(IC<53 + e>{}); });
    }
    post<6>(fme, rWs, tag);
    lds_bar();
    bf16x4 zl[5], zh[5];
    auto rdL = [&](auto f_) { constexpr int f = f_, j = f / 5, t = f % 5; constexpr unsigned z = ZU[j & 7], k = z / 57344; zl[t] = trrd(aZ[k][t & 1] + (z - k * 57344 + t * 32)); };
    auto rdH = [&](auto f_) { constexpr int f = f_, j = f / 5, t = f % 5; constexpr unsigned z = ZU[j & 7], k = z / 57344; zh[t] = trrd(aZ[k][t & 1] + (z - k * 57344 + t * 32 + 5120)); };
    f32x4 r[5][3];
    static_for<3>([&](auto u) { fb[0][u] = *(LAS bf16x8*)(aO[0] + u * 1024); });
    static_for<5>([&](auto t) { rdL(t); if constexpr (t < 4) rdH(t); });
    SB();
    auto step = [&](auto j_) { constexpr int j = j_, n = j + 1;
      static_for<15>([&](auto m_) { constexpr int m = m_, t = m / 3, u = m % 3, f = 5 * j + t;
        r[t][u] = mfma(cat(zl[t], zh[t]), fb[j & 1][u], j == 0 ? f32x4{0, 0, 0, 0} : r[t][u]); SB();
        if constexpr (u == 0) rdH(IC<f + 4>{});
        if constexpr (u == 1 && t < 3) { if constexpr (n < 8) fb[n & 1][t] = *(LAS bf16x8*)(aO[n] + t * 1024); else fb[n & 1][t] = *(LAS bf16x8*)(aF[(n >> 2) & 1] + ((n & 3) * 6144 + t * 1024)); }
        if constexpr (u == 2) rdL(IC<f + 5>{});
        SB(); }); };
    static_for<8>([&](auto j) { if constexpr (j > 0) bar();
      if constexpr (j == 7) { post<1>(fme + WS_FO2, rWs, tag); SB(); }
      step(j); });
#pragma unroll 1
    for (unsigned it = 0; it < 3; ++it) static_for<8>([&](auto j) { bar(); step(IC<8 + j>{}); });
    const unsigned pbase = WS_P + (cluster * 4 + hp) * 16 * 8192;
    if (w != hg) {
      static_for<8>([&](auto k) { const bf16x8 v = k < 6 ? pack8(r[2 * (k & 1)][k / 2], r[2 * (k & 1) + 1][k / 2]) : k == 6 ? pack8(r[4][0], r[4][1]) : pack8(r[4][2], r[4][2]);
        st_sc1<(k & 3) * 1024>(v, l16, rWs, pbase + (w * 4 + hg) * 8192 + (k >> 2) * 4096); });
      post<0>(WS_FP + (((cluster * 4 + hp) * 4 + w) * 4 + hg) * 128, rWs, tag);
    } else {
      static_for<15>([&](auto i) { constexpr int t = i / 3, u = i % 3, k = t < 4 ? 2 * u + (t >> 1) : 6 + (u >> 1), h = t < 4 ? (t & 1) : (u & 1);
        *(LAS f32x4*)(L_OWN + (2 * k + h) * 1024 + l16) = r[t][u]; });
    }
    lds_bar();
    f32x4 o[4];
    static_for<4>([&](auto j) { o[j] = *(LAS f32x4*)(L_OWN + (4 * w + j) * 1024 + l16); });
    flag_spin(WS_FP + (((cluster * 4 + hp) * 4 + hg) * 4 + ((hg + 1 + lane % 3u) & 3u)) * 128, rWs, tag);
    bf16x8 in[3][2];
    static_for<3>([&](auto s) { const unsigned src = (hg + 1 + s) & 3u; static_for<2>([&](auto k) { ld_sc1<k * 1024>(in[s][k], l16, rWs, pbase + (hg * 4 + src) * 8192 + w * 2048); }); });
    asm volatile("s_waitcnt vmcnt(0)" : "+v"(in[0][0]), "+v"(in[0][1]), "+v"(in[1][0]), "+v"(in[1][1]), "+v"(in[2][0]), "+v"(in[2][1]) :: "memory");
    static_for<3>([&](auto s) { static_for<2>([&](auto k) { const u32x4 v = __builtin_bit_cast(u32x4, in[s][k]);
      static_for<2>([&](auto h) { o[2 * k + h] += f32x4{__uint_as_float(v[2 * h] << 16), __uint_as_float(v[2 * h] & 0xffff0000u), __uint_as_float(v[2 * h + 1] << 16), __uint_as_float(v[2 * h + 1] & 0xffff0000u)}; }); }); });
    const rsrc_t rO = mkrsrc(pout, 768 * 1280 * 2);
    const unsigned ov = c * 2560, os = ((b * 96 + 48 * (hg & 1u)) * 1280 + br * 640 + hp * 160 + (hg >> 1) * 80) * 2;
    if (w < 3) {
      static_for<2>([&](auto k) {
        unsigned a0 = cvtpk(o[2 * k][0], o[2 * k][1]), a1 = cvtpk(o[2 * k][2], o[2 * k][3]), b0 = cvtpk(o[2 * k + 1][0], o[2 * k + 1][1]), b1 = cvtpk(o[2 * k + 1][2], o[2 * k + 1][3]);
        auto r0 = __builtin_amdgcn_permlane16_swap(a0, b0, false, false); auto r1 = __builtin_amdgcn_permlane16_swap(a1, b1, false, false);
        const u32x4 v = {r0[0], r1[0], r0[1], r1[1]};
        st_out(v, ov + k * 64 + (g & 1u) * 32 + (g >> 1) * 16, rO, os + w * 16 * 2560); });
    } else {
      static_for<3>([&](auto u) { const u32x2 v = {cvtpk(o[u][0], o[u][1]), cvtpk(o[u][2], o[u][3])}; __builtin_amdgcn_raw_buffer_store_b64(v, rO, ov + 128 + g * 8, os + u * 16 * 2560, 0); });
    }
    if ((hg | hp | w) == 0) post<63>(WS_EPOCH + cluster * 256, rWs, tag);
    asm volatile("fused_text_end:");
    return;
  }
  const char *px, *pk, *pv, *pwq, *pwo; char* ws;
  { const void* ka = (const void*)__builtin_amdgcn_kernarg_segment_ptr(); const unsigned o = br * 8;
    asm volatile("s_load_dwordx2 %0, %6, %7 offset:0x30\n\ts_load_dwordx2 %1, %6, %7 offset:0x0\n\ts_load_dwordx2 %2, %6, 0x58\n\ts_load_dwordx2 %3, %6, %7 offset:0x10\n\t"
                 "s_load_dwordx2 %4, %6, %7 offset:0x20\n\ts_load_dwordx2 %5, %6, %7 offset:0x40"
                 : "=&s"(pwq), "=&s"(px), "=&s"(ws), "=&s"(pk), "=&s"(pv), "=&s"(pwo) : "s"(ka), "s"(o) : "memory"); }
  SB();
  unsigned vWq[4], vX[3];
  static_for<4>([&](auto j) { const unsigned row = 8 * w + 2 * j + (lane >> 5), f = (row & 3u) | ((w & 1u) << 2); vWq[j] = row * 8192 + (((lane & 31u) ^ (f << 1)) << 4) - 1024 * j; });
  static_for<3>([&](auto j) { const unsigned row = 24 * w + 8 * j + (lane >> 3); vX[j] = row * 1280 + (((lane & 7u) ^ ((row >> 1) & 7u)) << 4) - 1024 * j; });
  const unsigned rot = (((b + 2 * hg) & 6u) * 5u) >> 2;
  const unsigned w4 = w * 4096, w3 = w * 3072, wk = w * 1024;
  unsigned soU = rot << 19, soE = rot << 7, mU = w4 + L_WQ, mE = w3 + L_X;
  asm volatile("" : "+v"(vWq[0]), "+v"(vWq[1]), "+v"(vWq[2]), "+v"(vWq[3]), "+v"(vX[0]), "+v"(vX[1]), "+v"(vX[2]), "+s"(soU), "+s"(soE), "+s"(mU), "+s"(mE));
  asm volatile("s_waitcnt lgkmcnt(0)" : "+s"(pwq), "+s"(px), "+s"(ws), "+s"(pk), "+s"(pv), "+s"(pwo) :: "memory");
  const rsrc_t rWq = mkrsrc(pwq + hA * DH * 2, DM * HD * 2 - hA * DH * 2);
  const rsrc_t rX  = mkrsrc(px + b * (SQ * DM * 2), SQ * DM * 2);
  auto issueU = [&] { const unsigned so = soU; dma<0>(vWq[0], rWq, so); SB();
    soU += 1u << 18; soU = soU == (20u << 18) ? 0u : soU; asm volatile("" : "+s"(soU)); SB(); dma<1024>(vWq[1], rWq, so); SB();
    mU = w4 + L_WQ + ((mU - w4 + 16384) & 65535u); asm volatile("" : "+s"(mU)); SB(); dma<2048>(vWq[2], rWq, so); dma<3072>(vWq[3], rWq, so); set_m0r(mE); };
  auto issueE01 = [&] { dma<0>(vX[0], rX, soE); dma<1024>(vX[1], rX, soE); set_m0r(mU); };
  auto issueE2 = [&] { dma<2048>(vX[2], rX, soE); set_m0r(mU);
    soE += 128; soE = soE == 1280u ? 0u : soE; mE += 12288; mE = mE == w3 + L_X + 36864 ? w3 + L_X : mE; asm volatile("" : "+s"(soE), "+s"(mE)); };
  unsigned junk = 0;
  set_m0r(mU); issueU(); issueE01(); set_m0r(mE); issueE2(); issueU();
  rsrc_t rC;
  {
    u64 pc; int len, len2; unsigned here;
    asm volatile("s_getpc_b64 %0\nfused_text_here:\n\ts_mov_b32 %1, fused_text_end - fused_text_begin\n\ts_mov_b32 %2, fused_text_end2 - fused_text_begin\n\ts_mov_b32 %3, fused_text_here - fused_text_begin"
                 : "=s"(pc), "=s"(len), "=s"(len2), "=s"(here));
    rC = mkrsrc((const char*)((pc - here) & ~127ull), len > len2 ? len : len2); }
  auto pf_text = [&] { touch(junk, (slot * 4 + w + (lane & 3u) * 128) * 128, rC); };
  pf_text();
  const u64 tag = sload64(ws + WS_EPOCH + cluster * 256) + 1;
  const rsrc_t rK  = mkrsrc(pk + (b * NH + hA) * (SQ * DH * 2), 2 * SQ * DH * 2);
  const rsrc_t rV  = mkrsrc(pv + (b * NH + hA) * (SQ * DH * 2), 2 * SQ * DH * 2);
  const rsrc_t rWo = mkrsrc(pwo + hg * (1024 * DM * 2), 1024 * DM * 2);
  const rsrc_t rWs = mkrsrc(ws, WS_END);
  const unsigned vK = (4 * w + g) * 256 + ((c ^ ((4 * w + g) & 15u)) << 4);
  const unsigned vV = (4 * w + g) * 256 + ((c ^ (((4 * w + g) & 7u) << 1)) << 4);
  const unsigned kp = 1u + ((slot + (slot >> 2)) & 3u);
  unsigned tab;
  { const unsigned j = lane & 31u; unsigned a = (j < 20 ? j - 8 : j - 20) + ((3 * b) >> 1); a = a >= 12u ? a - 12u : a;
    const unsigned rw = j < 8 ? 8 * hp + ((j + b) & 7u) : (2 * ((hp + 1 + (a >> 2)) & 3u) + ((a >> 1) & 1u)) * 4 + (j >= 20 ? 2u : 0u) + (a & 1u);
    tab = par == 0 ? rw * (32 * 1280) : WS_O + ((cluster * 32 + 8 * hg + (rw >> 2)) * 2 + qh) * 12288 + (rw & 3u) * 3072; }
  unsigned vWo[5];
  static_for<5>([&](auto i) { const unsigned e = i * 64 + lane, row = e / 20, pos = e - row * 20; vWo[i] = (row + 16 * qh) * 1280 + hp * 320 + ((pos ^ (((row >> 2) & 1u) << 1)) << 4) - 1024 * (i & 3); });
  asm volatile("" :: "s"(rK), "s"(rV), "s"(rWo), "s"(rWs), "v"(vK), "v"(vV), "v"(tab), "v"(vWo[0]), "v"(vWo[1]), "v"(vWo[2]), "v"(vWo[3]), "v"(vWo[4]));
  auto pf_half = [&](unsigned half, unsigned i, unsigned n, unsigned l0) { const unsigned l = i * n + (l0 + lane < n ? l0 + lane : n - 1), h = l / 640u; touch(junk, (l + h * 640u + half * 640u) * 128, rWo); };
  set_m0r(mE); issueE01(); set_m0r(mE); issueE2(); issueU();
  vm_bar<12>();
  vm_bar<8>();  set_m0r(mU); issueU(); issueE01(); set_m0r(mE); issueE2(); issueU();
  vm_bar<11>(); issueE01(); issueU();
  auto pair = [&](auto n0, auto n1, auto burst) { vm_bar<n0>(); issueE2(); issueU();
    if constexpr (burst) { static_for<12>([&](auto j) { dma_nt(wk + (L_K + 4096 * j), vK, rK, j * 4096); }); set_m0r(mE); }
    vm_bar<n1>(); issueE01(); issueU(); };
#pragma unroll 1
  for (unsigned i = 0; i < kp; ++i) pair(IC<11>{}, IC<11>{}, IC<0>{});
  pair(IC<11>{}, IC<23>{}, IC<1>{}); pair(IC<23>{}, IC<23>{}, IC<0>{});
#pragma unroll 1
  for (unsigned i = kp + 2; i < 6; ++i) pair(IC<11>{}, IC<11>{}, IC<0>{});
  vm_bar<11>(); issueE2(); issueU(); pf_text();
  vm_bar<12>(); set_m0r(mU); issueU();
  static_for<3>([&](auto i) { vm_bar<10 - i>(); static_for<4>([&](auto j) { constexpr int q = 4 * i + j; dma_nt(wk + (L_V + 4096 * q), vV, rV, q * 4096); }); });
  bar();
  pf_half(0, slot * 4 + w, 40, 0);
  wait_vm<7>(); cnt_add(L_CNT);
  if (par == 0) {
    const unsigned zq = qh * 5120;
    auto issueZ = [&](unsigned j, unsigned lds) { const unsigned so = rdlane(tab, j);
      set_m0r(lds); static_for<4>([&](auto k) { dma<1024 * k>(vWo[k], rWo, so); }); set_m0r(lds + 4096); dma<0>(vWo[4], rWo, so); };
    issueZ(0, zq + ZU[0]); issueZ(1, zq + ZU[1]); issueZ(2, zq + ZU[2]);
    wait_vm<16>(); cnt_add(L_CNT + 4);
    issueZ(3, zq + ZU[3]); issueZ(4, zq + ZU[4]); issueZ(5, zq + ZU[5]);
    bf16x8 zr[2][5];
    static_for<2>([&](auto k) { const unsigned so = rdlane(tab, 6 + k); static_for<5>([&](auto i) { ld_plain<1024 * (i & 3)>(zr[k][i], vWo[i], rWo, so); }); });
    vm_bar<30>();
    auto putZ = [&](auto a, auto n) { static_for<n>([&](auto q) { constexpr int e = a + q; *(LAS bf16x8*)(ZU[6 + e / 5] + zq + (e % 5) * 1024 + l16) = zr[e / 5][e % 5]; }); };
    vm_bar<25>();
    asm volatile("s_waitcnt vmcnt(0)" : "+v"(zr[0][0]), "+v"(zr[0][1]), "+v"(zr[0][2]), "+v"(zr[0][3]), "+v"(zr[0][4]), "+v"(zr[1][0]), "+v"(zr[1][1]), "+v"(zr[1][2]), "+v"(zr[1][3]), "+v"(zr[1][4]) :: "memory");
    putZ(IC<0>{}, IC<2>{});
    bar();        putZ(IC<2>{}, IC<3>{});
    bar();        issueZ(8, zq + ZU[0]); pf_half(1, slot * 2 + qh, 80, 0); pf_half(1, slot * 2 + qh, 80, 64);
    bar();        putZ(IC<5>{}, IC<3>{});
    lds_bar();    putZ(IC<8>{}, IC<2>{});
    lds_bar();    issueZ(9, zq + ZU[1]); issueZ(10, zq + ZU[2]);
    vm_bar<12>(); issueZ(11, zq + ZU[3]); issueZ(12, zq + ZU[4]);
    vm_bar<15>(); issueZ(13, zq + ZU[5]); issueZ(14, zq + ZU[6]);
    vm_bar<20>(); issueZ(15, zq + ZU[7]);
    unsigned so = rdlane(tab, 16);
    set_m0(zq, ZU[0]);
#pragma unroll 1
    for (unsigned it = 0; it < 2; ++it) static_for<8>([&](auto k) {
      vm_bar<20>();
      dma<0>(vWo[0], rWo, so); SB();
      unsigned sn = __builtin_amdgcn_readlane(tab, 8 * it + k + 17); asm volatile("" : "+s"(sn)); SB();
      dma<1024>(vWo[1], rWo, so); dma<2048>(vWo[2], rWo, so); dma<3072>(vWo[3], rWo, so);
      set_m0(zq, ZU[k] + 4096); asm volatile("s_nop 0"); dma<0>(vWo[4], rWo, so); set_m0(zq, ZU[(k + 1) & 7]);
      so = sn; });
    vm_bar<20>(); vm_bar<15>(); vm_bar<10>(); vm_bar<5>(); vm_bar<0>(); bar();
  } else {
    const unsigned fL = WS_FO + ((cluster * 4 + hg) * 2 + qh) * 128, fA = fL + (lane & 7u) * 8, q3 = qh * 3072;
    unsigned so;
    auto issueO = [&](unsigned kn, auto sl) {
      dma_sc1<0>(l16, rWs, so); SB(); unsigned sn = __builtin_amdgcn_readlane(tab, kn); asm volatile("" : "+s"(sn)); SB();
      dma_sc1<1024>(l16, rWs, so); dma_sc1<2048>(l16, rWs, so); set_m0(q3, oslot(sl)); asm volatile("s_nop 1"); so = sn; };
    const unsigned s8 = rdlane(tab, 8), s9 = rdlane(tab, 9);
    so = rdlane(tab, 10);
    u32x16 pk16 = {}; unsigned got = 0;
    asm volatile("" : "+{s[80:95]}"(pk16));
    wait_vm<1>(); cnt_add(L_CNT + 4);
    bar();
    static_for<6>([&](auto j0) { constexpr int j = j0 + 1; bar();
      if constexpr (j == 3) peek_issue(pk16, ws + fL);
      if constexpr (j > 3) peek_step<(j < 6)>(pk16, got, tag, ws + fL, l16, rWs, s8, s9, q3 + oslot(8), q3 + oslot(9)); });
    if (!got) { flag_spin(fA, rWs, tag);
      set_m0r(q3 + oslot(8)); static_for<3>([&](auto u) { dma_sc1<1024 * u>(l16, rWs, s8); }); set_m0r(q3 + oslot(9)); static_for<3>([&](auto u) { dma_sc1<1024 * u>(l16, rWs, s9); }); }
    set_m0r(q3 + oslot(10)); issueO(11, IC<11>{}); issueO(12, IC<12>{});
    vm_bar<9>();
    unsigned miss = 0;
    static_for<8>([&](auto i) { constexpr int j = 8 + i; vm_bar<6>(); issueO(j + 5, IC<j + 5>{});
      if constexpr (j == 13) peek_issue(pk16, ws + fL + WS_FO2); if constexpr (j == 15) miss = peek_check(pk16, tag); });
    vm_bar<6>();
    if (miss) flag_spin(fA + WS_FO2, rWs, tag);
    issueO(21, IC<21>{});
    static_for<11>([&](auto i) { constexpr int j = 17 + i; vm_bar<6>(); issueO(j + 5, IC<j + 5>{}); });
    vm_bar<6>(); vm_bar<3>(); vm_bar<0>(); bar();
  }
  asm volatile("s_waitcnt vmcnt(0)\nfused_text_end2:" : "+v"(junk) :: "memory");
  bar();
}

extern "C" void kernel_launch(void* const* d_in, const int* in_sizes, int n_in, void* d_out, int out_size, void* d_ws, size_t ws_size, hipStream_t stream) {
  Params p;
  for (int i = 0; i < 2; ++i) { p.x[i] = (const char*)d_in[5 * i]; p.k[i] = (const char*)d_in[5 * i + 1]; p.v[i] = (const char*)d_in[5 * i + 2]; p.wq[i] = (const char*)d_in[5 * i + 3]; p.wo[i] = (const char*)d_in[5 * i + 4]; }
  p.out = (char*)d_out; p.ws = (char*)d_ws;
  (void)hipFuncSetAttribute((const void*)fused, hipFuncAttributeMaxDynamicSharedMemorySize, LDS_BYTES);
  fused<<<256, 512, LDS_BYTES, stream>>>(p);
}
```
